```python
import torch

def _softmax(x, axis=-1):
    return torch.softmax(x, dim=axis)
def _gelu(x):
    return torch.nn.functional.gelu(x, approximate="tanh")
def _silu(x):
    return torch.nn.functional.silu(x)
def _sigmoid(x):
    return torch.sigmoid(x)
def _layernorm(x, axis=-1, eps=1e-5):
    m = x.mean(dim=axis, keepdim=True)
    v = x.var(dim=axis, keepdim=True, unbiased=False)
    return (x - m) / torch.sqrt(v + eps)
def _scatter_add(tgt, idx, src, axis=0):
    return tgt.clone().index_add_(axis, idx, src)
def _segment_sum(src, seg, n):
    out = torch.zeros((n,) + src.shape[1:], dtype=src.dtype)
    return out.index_add_(0, seg, src)
def _pad(x, pw):
    flat = tuple(v for pair in reversed(pw) for v in pair)
    return torch.nn.functional.pad(x, flat)

kernel_name = 'sequential_preLN__ssm_conv_h3__dense_activation_relu__prompt'

def setup_inputs():

    _w = (lambda a: (lambda t: t.to(torch.bfloat16) if t.is_floating_point() else t)(torch.as_tensor(a)))
    def _A_setup():
        B, S, D = 1, 2048, 512
        ks = 4
        return {
            "q": _w((lambda *s: torch.randn(*s, dtype=torch.bfloat16) * 0.05)(B, S, D)),
            "k": _w((lambda *s: torch.randn(*s, dtype=torch.bfloat16) * 0.4)(B, S, D)),
            "v": _w((lambda *s: torch.randn(*s, dtype=torch.bfloat16) * 0.4)(B, S, D)),
            "shift_k": _w((lambda *s: torch.randn(*s, dtype=torch.bfloat16) * 0.4)(D, ks) * 0.5),
            "ssm_k": _w((lambda *s: torch.randn(*s, dtype=torch.bfloat16) * 0.4)(D, S) * (1.0 / S ** 0.5)),
            "ssm_D": _w((lambda *s: torch.randn(*s, dtype=torch.bfloat16) * 0.4)(D)),
        }
    _A_in = _A_setup()

    def _B_setup():
        B, S, D, Dff = 1, 2048, 512, 1792
        return {
            "x": _w((lambda *s: torch.randn(*s, dtype=torch.bfloat16) * 0.05)(B, S, D)),
            "w1": _w((lambda *s: torch.randn(*s, dtype=torch.bfloat16) * 0.05)(D, Dff)),
            "w2": _w((lambda *s: torch.randn(*s, dtype=torch.bfloat16) * 0.05)(Dff, D)),
        }
    _B_in = _B_setup()

    return {
        "A_q": _A_in["q"],
        "A_k": _A_in["k"],
        "A_v": _A_in["v"],
        "A_shift_k": _A_in["shift_k"],
        "A_ssm_k": _A_in["ssm_k"],
        "A_ssm_D": _A_in["ssm_D"],
        "B_x": _B_in["x"],
        "B_w1": _B_in["w1"],
        "B_w2": _B_in["w2"],
    }

def reference(A_q, A_k, A_v, A_shift_k, A_ssm_k, A_ssm_D, B_x, B_w1, B_w2):
    def _A_ref(q, k, v, shift_k, ssm_k, ssm_D):
        B, S, D = q.shape
        ks = shift_k.shape[-1]
        k_pad = _pad(k, ((0, 0), (ks - 1, 0), (0, 0)))
        k_shift = torch.zeros_like(k)
        for t in range(ks):
            k_shift = k_shift + k_pad[:, t:t + S, :] * shift_k[:, ks - 1 - t][None, None, :]
        kv = k_shift * v
        L = 2 * S
        kv_pad = _pad(kv, ((0, 0), (0, S), (0, 0)))
        kf_pad = _pad(ssm_k, ((0, 0), (0, S)))
        kv_f = (lambda x, axes=(-2,-1): torch.fft.rfft2(x.float(), dim=axes))(kv_pad[:, None, :, :], axes=(1, 2))
        kf_f = (lambda x, axes=(-2,-1): torch.fft.rfft2(x.float(), dim=axes))(kf_pad[:, None, :], axes=(1, 2))
        kf_f = (lambda x, *d: x.permute(*d))(kf_f, 1, 2, 0)
        y_f = kv_f * kf_f[None, :, :, :]
        y = (lambda x, s, axes=(-2,-1): torch.fft.irfft2(x, s=s, dim=axes).to(torch.bfloat16))(y_f, (1, L), axes=(1, 2))[:, 0, :S, :]
        y = y + kv * ssm_D[None, None, :]
        return q * y

    def _B_ref(x, w1, w2):
        h = torch.einsum("bsd,df->bsf", x, w1)
        zero = torch.zeros_like(h)
        h = torch.maximum(h, zero)
        return torch.einsum("bsf,fd->bsd", h, w2)

    x = A_q
    h = x + _A_ref(_layernorm(x, axis=-1), A_k, A_v, A_shift_k, A_ssm_k, A_ssm_D)
    h2 = h + _B_ref(_layernorm(h, axis=-1), B_w1, B_w2)
    return h2
```

```cpp
#include <hip/hip_runtime.h>
#include <hip/hip_bf16.h>

typedef unsigned v4u __attribute__((ext_vector_type(4)));
typedef unsigned v2u __attribute__((ext_vector_type(2)));
typedef float f4 __attribute__((ext_vector_type(4)));
typedef float f2 __attribute__((ext_vector_type(2)));
typedef float f16v __attribute__((ext_vector_type(16)));
typedef __bf16 bf8 __attribute__((ext_vector_type(8)));
typedef __bf16 bf2 __attribute__((ext_vector_type(2)));
typedef short s4 __attribute__((ext_vector_type(4)));
typedef short s2 __attribute__((ext_vector_type(2)));
#define LAS __attribute__((address_space(3)))
#define GAS __attribute__((address_space(1)))

constexpr int S_ = 2048, D_ = 512, F_ = 1792;
constexpr unsigned OFF_U = 0x10000, OFF_Q2 = OFF_U + (2u << 20), OFF_ST1 = OFF_Q2 + (2u << 20), OFF_HB = OFF_ST1 + 0x8000,
                   OFF_ST2 = OFF_HB + (2u << 20), OFF_HH = OFF_ST2 + 0x8000, WS_END = OFF_HH + 32u * 112u * 2048u;

__device__ __forceinline__ int pos2tok(int p) { return (p & ~255) | (((p >> 2) & 15) << 4) | (((p >> 6) & 3) << 2) | (p & 3); }
__device__ __forceinline__ float bflo(unsigned u) { return __builtin_bit_cast(float, u << 16); }
__device__ __forceinline__ float bfhi(unsigned u) { return __builtin_bit_cast(float, u & 0xffff0000u); }
__device__ __forceinline__ unsigned pk(float lo, float hi) { bf2 v = {(__bf16)lo, (__bf16)hi}; return __builtin_bit_cast(unsigned, v); }
__device__ __forceinline__ void dma16(const void* g, LAS char* l) { __builtin_amdgcn_global_load_lds((const GAS unsigned*)g, (LAS unsigned*)l, 16, 0, 0); }
__device__ __forceinline__ v2u trrd(LAS char* p) { return __builtin_bit_cast(v2u, __builtin_amdgcn_ds_read_tr16_b64_v4i16((LAS s4*)p)); }
__device__ __forceinline__ f4 mfma16(v4u a, v4u b, f4 c) { return __builtin_amdgcn_mfma_f32_16x16x32_bf16(__builtin_bit_cast(bf8, a), __builtin_bit_cast(bf8, b), c, 0, 0, 0); }
__device__ __forceinline__ f16v mfma32(v4u a, v4u b, f16v c) { return __builtin_amdgcn_mfma_f32_32x32x16_bf16(__builtin_bit_cast(bf8, a), __builtin_bit_cast(bf8, b), c, 0, 0, 0); }
__device__ __forceinline__ float wsum(float v) {
#pragma unroll
  for (int m = 1; m < 64; m <<= 1) v += __shfl_xor(v, m);
  return v;
}

__global__ void __launch_bounds__(512) k_p0(const char* __restrict__ q, const char* __restrict__ k, const char* __restrict__ v,
                                            const char* __restrict__ sk, char* __restrict__ ws) {
  __shared__ __attribute__((aligned(16))) char sm_[25 * 1040];
  LAS char* sm = (LAS char*)sm_;
  const int b = blockIdx.x, x = b & 7, i = b >> 3, t = threadIdx.x, l = t & 63, w = __builtin_amdgcn_readfirstlane(t >> 6);
  LAS char* sK = sm; LAS char* sV = sm + 9 * 1040; LAS char* sQ = sm + 17 * 1040;
  const int co = x * 128 + (l & 7) * 16, r8 = l >> 3;
  if (w < 4) {
    dma16(k + (size_t)(64 * i + 8 * w + r8) * 1024 + co, sK + (w + 1) * 1040);
    dma16(k + (size_t)(64 * i + 8 * w + 32 + r8) * 1024 + co, sK + (w + 5) * 1040);
    dma16(v + (size_t)(64 * i + 8 * w + r8) * 1024 + co, sV + w * 1040);
    dma16(v + (size_t)(64 * i + 8 * w + 32 + r8) * 1024 + co, sV + (w + 4) * 1040);
    if (w == 0) dma16(k + (size_t)max(64 * i - 8 + r8, 0) * 1024 + co, sK);
    const int o = t & 7, cp = t >> 3;
    const v4u sv = *(const v4u*)(sk + (64 * x + 2 * cp) * 8);
    asm volatile("s_waitcnt vmcnt(0)" ::: "memory");
    __syncthreads();
    unsigned kk[11], vv[8];
#pragma unroll
    for (int j = 0; j < 3; ++j) kk[j] = *(LAS unsigned*)(sK + o * 1040 + (5 + j) * 128 + cp * 4);
#pragma unroll
    for (int j = 0; j < 8; ++j) kk[3 + j] = *(LAS unsigned*)(sK + (o + 1) * 1040 + j * 128 + cp * 4);
#pragma unroll
    for (int j = 0; j < 8; ++j) vv[j] = *(LAS unsigned*)(sV + o * 1040 + j * 128 + cp * 4);
    if (i == 0 && o == 0) { kk[0] = 0; kk[1] = 0; kk[2] = 0; }
    const f2 s0 = {bflo(sv.x), bflo(sv.z)}, s1 = {bfhi(sv.x), bfhi(sv.z)}, s2_ = {bflo(sv.y), bflo(sv.w)}, s3 = {bfhi(sv.y), bfhi(sv.w)};
    f2 kf[11], r[8];
#pragma unroll
    for (int j = 0; j < 11; ++j) kf[j] = f2{bflo(kk[j]), bfhi(kk[j])};
#pragma unroll
    for (int j = 0; j < 8; ++j) r[j] = (kf[j] * s3 + kf[j + 1] * s2_ + kf[j + 2] * s1 + kf[j + 3] * s0) * f2{bflo(vv[j]), bfhi(vv[j])};
    v4u u0 = {pk(r[0].x, r[1].x), pk(r[2].x, r[3].x), pk(r[4].x, r[5].x), pk(r[6].x, r[7].x)};
    v4u u1 = {pk(r[0].y, r[1].y), pk(r[2].y, r[3].y), pk(r[4].y, r[5].y), pk(r[6].y, r[7].y)};
    char* up = ws + OFF_U + (64 * x + 2 * cp) * 4096 + i * 128 + o * 16;
    *(v4u*)up = u0; *(v4u*)(up + 4096) = u1;
  } else {
    const int wb = w - 4, tb = t - 256;
    dma16(q + (size_t)pos2tok(64 * i + 8 * wb + r8) * 1024 + co, sQ + wb * 1040);
    dma16(q + (size_t)pos2tok(64 * i + 8 * wb + 32 + r8) * 1024 + co, sQ + (wb + 4) * 1040);
    const int p0 = 8 * (32 * x + i) + 2 * wb;
    v4u qr[2];
#pragma unroll
    for (int j = 0; j < 2; ++j) qr[j] = *(const v4u*)(q + (size_t)pos2tok(p0 + j) * 1024 + l * 16);
    asm volatile("s_waitcnt vmcnt(0)" ::: "memory");
    __syncthreads();
    const int quad = tb & 15;
#pragma unroll
    for (int j = 0; j < 2; ++j) {
      const int cp = (tb >> 4) + 16 * j;
      v4u o;
#pragma unroll
      for (int e = 0; e < 4; ++e) o[e] = *(LAS unsigned*)(sQ + (quad >> 1) * 1040 + (4 * (quad & 1) + e) * 128 + cp * 4);
      *(v4u*)(ws + OFF_Q2 + (32 * x + cp) * 8192 + i * 256 + quad * 16) = o;
    }
#pragma unroll
    for (int j = 0; j < 2; ++j) {
      float f[8];
#pragma unroll
      for (int e = 0; e < 4; ++e) { f[2 * e] = bflo(qr[j][e]); f[2 * e + 1] = bfhi(qr[j][e]); }
      float s = 0;
#pragma unroll
      for (int e = 0; e < 8; ++e) s += f[e];
      const float mu = wsum(s) * (1.f / 512);
      float ss = 0;
#pragma unroll
      for (int e = 0; e < 8; ++e) ss += (f[e] - mu) * (f[e] - mu);
      const float rs = rsqrtf(wsum(ss) * (1.f / 512) + 1e-5f);
      if (l == 0) *(f2*)(ws + OFF_ST1 + (p0 + j) * 8) = f2{mu, rs};
    }
  }
}

constexpr int G_STR = 4096 + 64, U_STR = 4096 + 512;
__global__ void __launch_bounds__(256) k_conv(const char* __restrict__ g, const unsigned short* __restrict__ Dv, char* __restrict__ ws) {
  __shared__ __attribute__((aligned(16))) char sm_[2 * G_STR + 2 * U_STR + 32768];
  LAS char* sG = (LAS char*)sm_; LAS char* sU = sG + 2 * G_STR; LAS char* sX = sU + 2 * U_STR;
  const int b = blockIdx.x, x = b & 7, i = b >> 3, cp = 32 * x + i, t = threadIdx.x, l = t & 63, w = __builtin_amdgcn_readfirstlane(t >> 6);
#pragma unroll
  for (int c = 0; c < 2; ++c) {
    v4u gv = *(const v4u*)(g + (size_t)(2 * cp + c) * 4096 + t * 16);
    if (t == 0) gv.x = (gv.x & 0xffff0000u) | (pk(bflo(gv.x) + bflo(Dv[2 * cp + c]), 0.f) & 0xffffu);
    *(LAS v4u*)(sG + c * G_STR + 32 + t * 16) = gv;
    *(LAS v4u*)(sU + c * U_STR + 512 + t * 16) = *(const v4u*)(ws + OFF_U + (2 * cp + c) * 4096 + t * 16);
    if (t < 32) *(LAS v4u*)(sU + c * U_STR + t * 16) = v4u{0, 0, 0, 0};
    if (t < 2) { *(LAS v4u*)(sG + c * G_STR + t * 16) = v4u{0, 0, 0, 0}; *(LAS v4u*)(sG + c * G_STR + 4128 + t * 16) = v4u{0, 0, 0, 0}; }
  }
  v4u qv[2]; f4 st[2][2];
#pragma unroll
  for (int j = 0; j < 2; ++j) {
    qv[j] = *(const v4u*)(ws + OFF_Q2 + cp * 8192 + (2 * w + j) * 1024 + l * 16);
    st[j][0] = *(const f4*)(ws + OFF_ST1 + ((2 * w + j) * 256 + 4 * l) * 8);
    st[j][1] = *(const f4*)(ws + OFF_ST1 + ((2 * w + j) * 256 + 4 * l) * 8 + 16);
  }
  __syncthreads();
  const int ch = w >> 1, cl0 = 4 * (w & 1), r = l & 15, q4 = l >> 4;
  v4u gf[4][8];
  {
    const unsigned sel = (r & 1) ? 0x01000302u : 0x03020504u;
    volatile LAS unsigned* gb = (volatile LAS unsigned*)(sG + ch * G_STR) + 8 + ((r + 9) >> 1) - 4 * q4 + 16 * cl0;
#pragma unroll
    for (int cl = 0; cl < 4; ++cl)
#pragma unroll
      for (int ii = 0; ii < 8; ++ii) {
        unsigned d[5];
#pragma unroll
        for (int j = 0; j < 5; ++j) d[j] = gb[16 * (8 * ii + cl) + j];
#pragma unroll
        for (int j = 0; j < 4; ++j) gf[cl][ii][j] = __builtin_amdgcn_perm(d[4 - j], d[3 - j], sel);
      }
  }
  f4 acc[8];
#pragma unroll
  for (int c = 0; c < 8; ++c) acc[c] = f4{0, 0, 0, 0};
  LAS char* ub = sU + ch * U_STR + 512 + (2 * r + q4 - 2 - 4 * cl0) * 16;
#pragma unroll
  for (int cl = 0; cl < 4; ++cl) {
    v4u uf[8];
#pragma unroll
    for (int j = 0; j < 8; ++j) uf[j] = *(LAS v4u*)(ub + (32 * j - 4 * cl) * 16);
#pragma unroll
    for (int j = 0; j < 8; ++j)
#pragma unroll
      for (int ii = 0; ii + j < 8; ++ii) acc[ii + j] = mfma16(gf[cl][ii], uf[j], acc[ii + j]);
  }
#pragma unroll
  for (int c = 0; c < 8; ++c) *(LAS f4*)(sX + (w * 8 + c) * 1024 + l * 16) = acc[c];
  __syncthreads();
#pragma unroll
  for (int j = 0; j < 2; ++j) {
    const int ct = 2 * w + j;
    const f4 y0 = *(LAS f4*)(sX + (0 * 8 + ct) * 1024 + l * 16) + *(LAS f4*)(sX + (1 * 8 + ct) * 1024 + l * 16);
    const f4 y1 = *(LAS f4*)(sX + (2 * 8 + ct) * 1024 + l * 16) + *(LAS f4*)(sX + (3 * 8 + ct) * 1024 + l * 16);
    v4u o;
#pragma unroll
    for (int e = 0; e < 4; ++e) {
      const float mu = st[j][e >> 1][2 * (e & 1)], rs = st[j][e >> 1][2 * (e & 1) + 1];
      const float q0 = bflo(qv[j][e]), q1 = bfhi(qv[j][e]);
      o[e] = pk(q0 + (q0 - mu) * rs * y0[e], q1 + (q1 - mu) * rs * y1[e]);
    }
    *(v4u*)(ws + OFF_HB + (2 * ct + (l >> 5)) * 131072 + cp * 512 + (l & 31) * 16) = o;
  }
}

__global__ void __launch_bounds__(256) k_st2(char* __restrict__ ws) {
  __shared__ f2 red[16][64];
  const int b = blockIdx.x, t = threadIdx.x, quad = t & 15, g = t >> 4;
  float s[4] = {0, 0, 0, 0}, ss[4] = {0, 0, 0, 0};
#pragma unroll
  for (int j = 0; j < 16; ++j) {
    const v4u h = *(const v4u*)(ws + OFF_HB + (b >> 1) * 131072 + (j * 16 + g) * 512 + (b & 1) * 256 + quad * 16);
#pragma unroll
    for (int e = 0; e < 4; ++e) { const float a = bflo(h[e]), c = bfhi(h[e]); s[e] += a + c; ss[e] += a * a + c * c; }
  }
#pragma unroll
  for (int e = 0; e < 4; ++e) red[g][quad * 4 + e] = f2{s[e], ss[e]};
  __syncthreads();
  if (t < 64) {
    f2 a = {0, 0};
#pragma unroll
    for (int j = 0; j < 16; ++j) a += red[j][t];
    const float mu = a.x * (1.f / 512), var = fmaxf(a.y * (1.f / 512) - mu * mu, 0.f);
    *(f2*)(ws + OFF_ST2 + (b * 64 + t) * 8) = f2{mu, rsqrtf(var + 1e-5f)};
  }
}

__global__ void __launch_bounds__(256) k_g1(const char* __restrict__ w1, char* __restrict__ ws) {
  extern __shared__ __attribute__((aligned(16))) char dsm_[];
  LAS char* sW = (LAS char*)dsm_; LAS char* sC = sW + 131072;
  const int b = blockIdx.x, x = b & 7, i = b >> 3, t = threadIdx.x, l = t & 63, w = __builtin_amdgcn_readfirstlane(t >> 6);
  if (i >= 28) return;
  const int panel = (x >> 1) * 4 + (i & 3), slice = (x & 1) * 7 + (i >> 2);
#pragma unroll
  for (int m = 0; m < 32; ++m) {
    const int row = (w * 32 + m) * 4 + (l >> 4);
    dma16(w1 + (size_t)row * 3584 + slice * 256 + ((l & 15) ^ ((l >> 4) << 2)) * 16, sW + (w * 32 + m) * 1024);
  }
  const int n = l & 31, hh = l >> 5;
  const char* hp = ws + OFF_HB + panel * 131072 + hh * 2048 + (32 * w + n) * 4;
  v4u hf[32];
#pragma unroll
  for (int s = 0; s < 32; ++s)
#pragma unroll
    for (int j = 0; j < 4; ++j) hf[s][j] = *(const unsigned*)(hp + s * 4096 + j * 512);
  const f2 st = *(const f2*)(ws + OFF_ST2 + (panel * 128 + 32 * w + n) * 8);
  asm volatile("s_waitcnt vmcnt(0)" ::: "memory");
  __syncthreads();
  const int a = (l >> 2) & 3, p = l & 3, fh = (l >> 4) & 1;
  LAS char* wb[4];
#pragma unroll
  for (int T = 0; T < 4; ++T) wb[T] = sW + (8 * hh + a) * 256 + (((4 * T + 2 * fh + (p >> 1)) ^ (a << 2)) * 16) + 8 * (p & 1);
  {
    f16v c = {};
    LAS char* wt = sW + (8 * hh + a) * 256 + (((4 * w + 2 * fh + (p >> 1)) ^ (a << 2)) * 16) + 8 * (p & 1);
    const v4u ones = {0x3f803f80u, 0x3f803f80u, 0x3f803f80u, 0x3f803f80u};
#pragma unroll
    for (int s = 0; s < 32; ++s) {
      const v2u lo = trrd(wt + s * 4096), hi = trrd(wt + s * 4096 + 1024);
      c = mfma32(v4u{lo.x, lo.y, hi.x, hi.y}, ones, c);
    }
    if (n == 0) {
#pragma unroll
      for (int g = 0; g < 4; ++g) *(LAS f4*)(sC + (w * 32 + 8 * g + 4 * hh) * 4) = f4{c[4 * g], c[4 * g + 1], c[4 * g + 2], c[4 * g + 3]};
    }
  }
  __syncthreads();
  f16v acc[4] = {};
#pragma unroll
  for (int s = 0; s < 32; ++s)
#pragma unroll
    for (int T = 0; T < 4; ++T) {
      const v2u lo = trrd(wb[T] + s * 4096), hi = trrd(wb[T] + s * 4096 + 1024);
      acc[T] = mfma32(v4u{lo.x, lo.y, hi.x, hi.y}, hf[s], acc[T]);
    }
  const int g32 = 4 * panel + w;
  char* op = ws + OFF_HH + ((g32 >> 1) * 112 + slice * 8) * 2048 + (g32 & 1) * 1024 + l * 16;
#pragma unroll
  for (int T = 0; T < 4; ++T)
#pragma unroll
    for (int s = 0; s < 2; ++s) {
      v4u o;
#pragma unroll
      for (int g = 0; g < 2; ++g) {
        const f4 c = *(LAS f4*)(sC + (T * 32 + 8 * (2 * s + g) + 4 * hh) * 4);
#pragma unroll
        for (int e = 0; e < 2; ++e) {
          const int rg = 8 * s + 4 * g + 2 * e;
          o[2 * g + e] = pk(fmaxf(acc[T][rg] - st.x * c[2 * e], 0.f), fmaxf(acc[T][rg + 1] - st.x * c[2 * e + 1], 0.f));
        }
      }
      *(v4u*)(op + (2 * T + s) * 2048) = o;
    }
}

__global__ void __launch_bounds__(256) k_g2(const char* __restrict__ w2, const char* __restrict__ ws, char* __restrict__ out) {
  extern __shared__ __attribute__((aligned(16))) char dsm_[];
  LAS char* sW = (LAS char*)dsm_;
  const int b = blockIdx.x, x = b & 7, i = b >> 3, t = threadIdx.x, l = t & 63, w = __builtin_amdgcn_readfirstlane(t >> 6);
  const int tt = 4 * x + (i & 3), cs = i >> 2;
  const int n = l & 31, hh = l >> 5, a = (l >> 2) & 3, p = l & 3, fh = (l >> 4) & 1;
  f16v acc[2][2] = {};
  LAS char* mw = sW + w * 28672;
  LAS char* wb[2];
#pragma unroll
  for (int T = 0; T < 2; ++T) wb[T] = mw + (4 * hh + a) * 128 + (((4 * T + 2 * fh + (p >> 1)) ^ (((a >> 1) & 1) << 2)) * 16) + 8 * (p & 1);
  for (int half = 0; half < 2; ++half) {
    const int ks0 = 28 * w + 14 * half;
#pragma unroll
    for (int m = 0; m < 28; ++m) {
      const int row = ks0 * 16 + m * 8 + (l >> 3);
      dma16(w2 + (size_t)row * 1024 + cs * 128 + ((l & 7) ^ (((l >> 4) & 1) << 2)) * 16, mw + m * 1024);
    }
    v4u hf[14][2];
#pragma unroll
    for (int s = 0; s < 14; ++s)
#pragma unroll
      for (int th = 0; th < 2; ++th) hf[s][th] = *(const v4u*)(ws + OFF_HH + (tt * 112 + ks0 + s) * 2048 + th * 1024 + l * 16);
    asm volatile("s_waitcnt vmcnt(0)" ::: "memory");
#pragma unroll
    for (int s = 0; s < 14; ++s)
#pragma unroll
      for (int T = 0; T < 2; ++T) {
        const v2u lo = trrd(wb[T] + s * 2048), hi = trrd(wb[T] + s * 2048 + 1024);
        const v4u wf = {lo.x, lo.y, hi.x, hi.y};
#pragma unroll
        for (int th = 0; th < 2; ++th) acc[th][T] = mfma32(wf, hf[s][th], acc[th][T]);
      }
    asm volatile("s_waitcnt lgkmcnt(0)" ::: "memory");
  }
  __syncthreads();
#pragma unroll
  for (int th = 0; th < 2; ++th)
#pragma unroll
    for (int T = 0; T < 2; ++T)
#pragma unroll
      for (int g = 0; g < 4; ++g)
        *(LAS f4*)(sW + (w * 64 + th * 32 + n) * 272 + (T * 32 + 8 * g + 4 * hh) * 4) =
            f4{acc[th][T][4 * g], acc[th][T][4 * g + 1], acc[th][T][4 * g + 2], acc[th][T][4 * g + 3]};
  __syncthreads();
#pragma unroll
  for (int j = 0; j < 2; ++j) {
    const int pl = (t >> 3) + 32 * j, c8 = t & 7, pos = tt * 64 + pl;
    f4 s0 = {0, 0, 0, 0}, s1 = {0, 0, 0, 0};
#pragma unroll
    for (int ww = 0; ww < 4; ++ww) { s0 += *(LAS f4*)(sW + (ww * 64 + pl) * 272 + c8 * 32); s1 += *(LAS f4*)(sW + (ww * 64 + pl) * 272 + c8 * 32 + 16); }
    const float rs = *(const float*)(ws + OFF_ST2 + pos * 8 + 4);
    unsigned hr[4];
#pragma unroll
    for (int e = 0; e < 4; ++e) hr[e] = *(const unsigned*)(ws + OFF_HB + (pos >> 7) * 131072 + (cs * 32 + c8 * 4 + e) * 512 + (pos & 127) * 4);
    v4u o = {pk(bflo(hr[0]) + rs * s0[0], bfhi(hr[0]) + rs * s0[1]), pk(bflo(hr[1]) + rs * s0[2], bfhi(hr[1]) + rs * s0[3]),
             pk(bflo(hr[2]) + rs * s1[0], bfhi(hr[2]) + rs * s1[1]), pk(bflo(hr[3]) + rs * s1[2], bfhi(hr[3]) + rs * s1[3])};
    *(v4u*)(out + (size_t)pos2tok(pos) * 1024 + cs * 128 + c8 * 16) = o;
  }
}

extern "C" void kernel_launch(void* const* d_in, const int* in_sizes, int n_in, void* d_out, int out_size, void* d_ws, size_t ws_size,
                              hipStream_t stream) {
  const char* q = (const char*)d_in[0]; const char* k = (const char*)d_in[1]; const char* v = (const char*)d_in[2];
  const char* sk = (const char*)d_in[3]; const char* g = (const char*)d_in[4]; const unsigned short* Dv = (const unsigned short*)d_in[5];
  const char* w1 = (const char*)d_in[7]; const char* w2 = (const char*)d_in[8];
  char* ws = (char*)d_ws;
  (void)hipFuncSetAttribute((const void*)k_g1, hipFuncAttributeMaxDynamicSharedMemorySize, 131072 + 512);
  (void)hipFuncSetAttribute((const void*)k_g2, hipFuncAttributeMaxDynamicSharedMemorySize, 114688);
  k_p0<<<256, 512, 0, stream>>>(q, k, v, sk, ws);
  k_conv<<<256, 256, 0, stream>>>(g, Dv, ws);
  k_st2<<<32, 256, 0, stream>>>(ws);
  k_g1<<<256, 256, 131072 + 512, stream>>>(w1, ws);
  k_g2<<<256, 256, 114688, stream>>>(w2, ws, (char*)d_out);
}
```

```cpp
#define MODE 0
#include <hip/hip_runtime.h>
#include <hip/hip_bf16.h>

typedef unsigned v4u __attribute__((ext_vector_type(4)));
typedef unsigned v2u __attribute__((ext_vector_type(2)));
typedef float f4 __attribute__((ext_vector_type(4)));
typedef float f2 __attribute__((ext_vector_type(2)));
typedef float f16v __attribute__((ext_vector_type(16)));
typedef __bf16 bf8 __attribute__((ext_vector_type(8)));
typedef __bf16 bf2 __attribute__((ext_vector_type(2)));
typedef short s4 __attribute__((ext_vector_type(4)));
typedef short s2 __attribute__((ext_vector_type(2)));
#define LAS __attribute__((address_space(3)))
#define GAS __attribute__((address_space(1)))

constexpr int S_ = 2048, D_ = 512, F_ = 1792;
constexpr unsigned OFF_U = 0x10000, OFF_Q2 = OFF_U + (2u << 20), OFF_ST1 = OFF_Q2 + (2u << 20), OFF_HB = OFF_ST1 + 0x8000,
                   OFF_ST2 = OFF_HB + (2u << 20), OFF_HH = OFF_ST2 + 0x8000, WS_END = OFF_HH + 32u * 112u * 2048u;

__device__ __forceinline__ int pos2tok(int p) { return (p & ~255) | (((p >> 2) & 15) << 4) | (((p >> 6) & 3) << 2) | (p & 3); }
__device__ __forceinline__ float bflo(unsigned u) { return __builtin_bit_cast(float, u << 16); }
__device__ __forceinline__ float bfhi(unsigned u) { return __builtin_bit_cast(float, u & 0xffff0000u); }
__device__ __forceinline__ unsigned pk(float lo, float hi) { bf2 v = {(__bf16)lo, (__bf16)hi}; return __builtin_bit_cast(unsigned, v); }
__device__ __forceinline__ void dma16(const void* g, LAS char* l) { __builtin_amdgcn_global_load_lds((const GAS unsigned*)g, (LAS unsigned*)l, 16, 0, 0); }
__device__ __forceinline__ v2u trrd(LAS char* p) { return __builtin_bit_cast(v2u, __builtin_amdgcn_ds_read_tr16_b64_v4i16((LAS s4*)p)); }
__device__ __forceinline__ f4 mfma16(v4u a, v4u b, f4 c) { return __builtin_amdgcn_mfma_f32_16x16x32_bf16(__builtin_bit_cast(bf8, a), __builtin_bit_cast(bf8, b), c, 0, 0, 0); }
__device__ __forceinline__ f16v mfma32(v4u a, v4u b, f16v c) { return __builtin_amdgcn_mfma_f32_32x32x16_bf16(__builtin_bit_cast(bf8, a), __builtin_bit_cast(bf8, b), c, 0, 0, 0); }
__device__ __forceinline__ float wsum(float v) {
#pragma unroll
  for (int m = 1; m < 64; m <<= 1) v += __shfl_xor(v, m);
  return v;
}

__global__ void __launch_bounds__(512) k_p0(const char* __restrict__ q, const char* __restrict__ k, const char* __restrict__ v,
                                            const char* __restrict__ sk, char* __restrict__ ws) {
  __shared__ __attribute__((aligned(16))) char sm_[25 * 1040];
  LAS char* sm = (LAS char*)sm_;
  const int b = blockIdx.x, x = b & 7, i = b >> 3, t = threadIdx.x, l = t & 63, w = __builtin_amdgcn_readfirstlane(t >> 6);
  const unsigned long long t0_ = __builtin_amdgcn_s_memrealtime();
  LAS char* sK = sm; LAS char* sV = sm + 9 * 1040; LAS char* sQ = sm + 17 * 1040;
  const int co = x * 128 + (l & 7) * 16, r8 = l >> 3;
  if (w < 4) {
    dma16(k + (size_t)(64 * i + 8 * w + r8) * 1024 + co, sK + (w + 1) * 1040);
    dma16(k + (size_t)(64 * i + 8 * w + 32 + r8) * 1024 + co, sK + (w + 5) * 1040);
    dma16(v + (size_t)(64 * i + 8 * w + r8) * 1024 + co, sV + w * 1040);
    dma16(v + (size_t)(64 * i + 8 * w + 32 + r8) * 1024 + co, sV + (w + 4) * 1040);
    if (w == 0) dma16(k + (size_t)max(64 * i - 8 + r8, 0) * 1024 + co, sK);
    const int o = t & 7, cp = t >> 3;
    const v4u sv = *(const v4u*)(sk + (64 * x + 2 * cp) * 8);
    asm volatile("s_waitcnt vmcnt(0)" ::: "memory");
    __syncthreads();
    if (t == 0) { unsigned long long* sp = (unsigned long long*)(ws + (128u << 20)) + b * 2; sp[0] = t0_; sp[1] = __builtin_amdgcn_s_memrealtime(); }
    unsigned kk[11], vv[8];
#pragma unroll
    for (int j = 0; j < 3; ++j) kk[j] = *(LAS unsigned*)(sK + o * 1040 + (5 + j) * 128 + cp * 4);
#pragma unroll
    for (int j = 0; j < 8; ++j) kk[3 + j] = *(LAS unsigned*)(sK + (o + 1) * 1040 + j * 128 + cp * 4);
#pragma unroll
    for (int j = 0; j < 8; ++j) vv[j] = *(LAS unsigned*)(sV + o * 1040 + j * 128 + cp * 4);
    if (i == 0 && o == 0) { kk[0] = 0; kk[1] = 0; kk[2] = 0; }
    const f2 s0 = {bflo(sv.x), bflo(sv.z)}, s1 = {bfhi(sv.x), bfhi(sv.z)}, s2_ = {bflo(sv.y), bflo(sv.w)}, s3 = {bfhi(sv.y), bfhi(sv.w)};
    f2 kf[11], r[8];
#pragma unroll
    for (int j = 0; j < 11; ++j) kf[j] = f2{bflo(kk[j]), bfhi(kk[j])};
#pragma unroll
    for (int j = 0; j < 8; ++j) r[j] = (kf[j] * s3 + kf[j + 1] * s2_ + kf[j + 2] * s1 + kf[j + 3] * s0) * f2{bflo(vv[j]), bfhi(vv[j])};
    v4u u0 = {pk(r[0].x, r[1].x), pk(r[2].x, r[3].x), pk(r[4].x, r[5].x), pk(r[6].x, r[7].x)};
    v4u u1 = {pk(r[0].y, r[1].y), pk(r[2].y, r[3].y), pk(r[4].y, r[5].y), pk(r[6].y, r[7].y)};
    char* up = ws + OFF_U + (64 * x + 2 * cp) * 4096 + i * 128 + o * 16;
    *(v4u*)up = u0; *(v4u*)(up + 4096) = u1;
  } else {
    const int wb = w - 4, tb = t - 256;
    dma16(q + (size_t)pos2tok(64 * i + 8 * wb + r8) * 1024 + co, sQ + wb * 1040);
    dma16(q + (size_t)pos2tok(64 * i + 8 * wb + 32 + r8) * 1024 + co, sQ + (wb + 4) * 1040);
    const int p0 = 8 * (32 * x + i) + 2 * wb;
    v4u qr[2];
#pragma unroll
    for (int j = 0; j < 2; ++j) qr[j] = *(const v4u*)(q + (size_t)pos2tok(p0 + j) * 1024 + l * 16);
    asm volatile("s_waitcnt vmcnt(0)" ::: "memory");
    __syncthreads();
    const int quad = tb & 15;
#pragma unroll
    for (int j = 0; j < 2; ++j) {
      const int cp = (tb >> 4) + 16 * j;
      v4u o;
#pragma unroll
      for (int e = 0; e < 4; ++e) o[e] = *(LAS unsigned*)(sQ + (quad >> 1) * 1040 + (4 * (quad & 1) + e) * 128 + cp * 4);
      *(v4u*)(ws + OFF_Q2 + (32 * x + cp) * 8192 + i * 256 + quad * 16) = o;
    }
#pragma unroll
    for (int j = 0; j < 2; ++j) {
      float f[8];
#pragma unroll
      for (int e = 0; e < 4; ++e) { f[2 * e] = bflo(qr[j][e]); f[2 * e + 1] = bfhi(qr[j][e]); }
      float s = 0;
#pragma unroll
      for (int e = 0; e < 8; ++e) s += f[e];
      const float mu = wsum(s) * (1.f / 512);
      float ss = 0;
#pragma unroll
      for (int e = 0; e < 8; ++e) ss += (f[e] - mu) * (f[e] - mu);
      const float rs = rsqrtf(wsum(ss) * (1.f / 512) + 1e-5f);
      if (l == 0) *(f2*)(ws + OFF_ST1 + (p0 + j) * 8) = f2{mu, rs};
    }
  }
}

constexpr int G_STR = 4096 + 64, U_STR = 4096 + 512;
__global__ void __launch_bounds__(256) k_conv(const char* __restrict__ g, const unsigned short* __restrict__ Dv, char* __restrict__ ws) {
  __shared__ __attribute__((aligned(16))) char sm_[2 * G_STR + 2 * U_STR + 32768];
  LAS char* sG = (LAS char*)sm_; LAS char* sU = sG + 2 * G_STR; LAS char* sX = sU + 2 * U_STR;
  const int b = blockIdx.x, x = b & 7, i = b >> 3, cp = 32 * x + i, t = threadIdx.x, l = t & 63, w = __builtin_amdgcn_readfirstlane(t >> 6);
#pragma unroll
  for (int c = 0; c < 2; ++c) {
    v4u gv = *(const v4u*)(g + (size_t)(2 * cp + c) * 4096 + t * 16);
    if (t == 0) gv.x = (gv.x & 0xffff0000u) | (pk(bflo(gv.x) + bflo(Dv[2 * cp + c]), 0.f) & 0xffffu);
    *(LAS v4u*)(sG + c * G_STR + 32 + t * 16) = gv;
    *(LAS v4u*)(sU + c * U_STR + 512 + t * 16) = *(const v4u*)(ws + OFF_U + (2 * cp + c) * 4096 + t * 16);
    if (t < 32) *(LAS v4u*)(sU + c * U_STR + t * 16) = v4u{0, 0, 0, 0};
    if (t < 2) { *(LAS v4u*)(sG + c * G_STR + t * 16) = v4u{0, 0, 0, 0}; *(LAS v4u*)(sG + c * G_STR + 4128 + t * 16) = v4u{0, 0, 0, 0}; }
  }
  v4u qv[2]; f4 st[2][2];
#pragma unroll
  for (int j = 0; j < 2; ++j) {
    qv[j] = *(const v4u*)(ws + OFF_Q2 + cp * 8192 + (2 * w + j) * 1024 + l * 16);
    st[j][0] = *(const f4*)(ws + OFF_ST1 + ((2 * w + j) * 256 + 4 * l) * 8);
    st[j][1] = *(const f4*)(ws + OFF_ST1 + ((2 * w + j) * 256 + 4 * l) * 8 + 16);
  }
  __syncthreads();
  const int ch = w >> 1, cl0 = 4 * (w & 1), r = l & 15, q4 = l >> 4;
  v4u gf[4][8];
  {
    const unsigned sel = (r & 1) ? 0x01000302u : 0x03020504u;
    volatile LAS unsigned* gb = (volatile LAS unsigned*)(sG + ch * G_STR) + 8 + ((r + 9) >> 1) - 4 * q4 + 16 * cl0;
#pragma unroll
    for (int cl = 0; cl < 4; ++cl)
#pragma unroll
      for (int ii = 0; ii < 8; ++ii) {
        unsigned d[5];
#pragma unroll
        for (int j = 0; j < 5; ++j) d[j] = gb[16 * (8 * ii + cl) + j];
#pragma unroll
        for (int j = 0; j < 4; ++j) gf[cl][ii][j] = __builtin_amdgcn_perm(d[4 - j], d[3 - j], sel);
      }
  }
  f4 acc[8];
#pragma unroll
  for (int c = 0; c < 8; ++c) acc[c] = f4{0, 0, 0, 0};
  LAS char* ub = sU + ch * U_STR + 512 + (2 * r + q4 - 2 - 4 * cl0) * 16;
#pragma unroll
  for (int cl = 0; cl < 4; ++cl) {
    v4u uf[8];
#pragma unroll
    for (int j = 0; j < 8; ++j) uf[j] = *(LAS v4u*)(ub + (32 * j - 4 * cl) * 16);
#pragma unroll
    for (int j = 0; j < 8; ++j)
#pragma unroll
      for (int ii = 0; ii + j < 8; ++ii) acc[ii + j] = mfma16(gf[cl][ii], uf[j], acc[ii + j]);
  }
#pragma unroll
  for (int c = 0; c < 8; ++c) *(LAS f4*)(sX + (w * 8 + c) * 1024 + l * 16) = acc[c];
  __syncthreads();
#pragma unroll
  for (int j = 0; j < 2; ++j) {
    const int ct = 2 * w + j;
    const f4 y0 = *(LAS f4*)(sX + (0 * 8 + ct) * 1024 + l * 16) + *(LAS f4*)(sX + (1 * 8 + ct) * 1024 + l * 16);
    const f4 y1 = *(LAS f4*)(sX + (2 * 8 + ct) * 1024 + l * 16) + *(LAS f4*)(sX + (3 * 8 + ct) * 1024 + l * 16);
    v4u o;
#pragma unroll
    for (int e = 0; e < 4; ++e) {
      const float mu = st[j][e >> 1][2 * (e & 1)], rs = st[j][e >> 1][2 * (e & 1) + 1];
      const float q0 = bflo(qv[j][e]), q1 = bfhi(qv[j][e]);
      o[e] = pk(q0 + (q0 - mu) * rs * y0[e], q1 + (q1 - mu) * rs * y1[e]);
    }
    *(v4u*)(ws + OFF_HB + (2 * ct + (l >> 5)) * 131072 + cp * 512 + (l & 31) * 16) = o;
  }
}

__global__ void __launch_bounds__(256) k_st2(char* __restrict__ ws) {
  __shared__ f2 red[16][64];
  const int b = blockIdx.x, t = threadIdx.x, quad = t & 15, g = t >> 4;
  float s[4] = {0, 0, 0, 0}, ss[4] = {0, 0, 0, 0};
#pragma unroll
  for (int j = 0; j < 16; ++j) {
    const v4u h = *(const v4u*)(ws + OFF_HB + (b >> 1) * 131072 + (j * 16 + g) * 512 + (b & 1) * 256 + quad * 16);
#pragma unroll
    for (int e = 0; e < 4; ++e) { const float a = bflo(h[e]), c = bfhi(h[e]); s[e] += a + c; ss[e] += a * a + c * c; }
  }
#pragma unroll
  for (int e = 0; e < 4; ++e) red[g][quad * 4 + e] = f2{s[e], ss[e]};
  __syncthreads();
  if (t < 64) {
    f2 a = {0, 0};
#pragma unroll
    for (int j = 0; j < 16; ++j) a += red[j][t];
    const float mu = a.x * (1.f / 512), var = fmaxf(a.y * (1.f / 512) - mu * mu, 0.f);
    *(f2*)(ws + OFF_ST2 + (b * 64 + t) * 8) = f2{mu, rsqrtf(var + 1e-5f)};
  }
}

__global__ void __launch_bounds__(256) k_g1(const char* __restrict__ w1, char* __restrict__ ws) {
  extern __shared__ __attribute__((aligned(16))) char dsm_[];
  LAS char* sW = (LAS char*)dsm_; LAS char* sC = sW + 131072;
  const int b = blockIdx.x, x = b & 7, i = b >> 3, t = threadIdx.x, l = t & 63, w = __builtin_amdgcn_readfirstlane(t >> 6);
  if (i >= 28) return;
  const int panel = (x >> 1) * 4 + (i & 3), slice = (x & 1) * 7 + (i >> 2);
#pragma unroll
  for (int m = 0; m < 32; ++m) {
    const int row = (w * 32 + m) * 4 + (l >> 4);
    dma16(w1 + (size_t)row * 3584 + slice * 256 + ((l & 15) ^ ((l >> 4) << 2)) * 16, sW + (w * 32 + m) * 1024);
  }
  const int n = l & 31, hh = l >> 5;
  const char* hp = ws + OFF_HB + panel * 131072 + hh * 2048 + (32 * w + n) * 4;
  v4u hf[32];
#pragma unroll
  for (int s = 0; s < 32; ++s)
#pragma unroll
    for (int j = 0; j < 4; ++j) hf[s][j] = *(const unsigned*)(hp + s * 4096 + j * 512);
  const f2 st = *(const f2*)(ws + OFF_ST2 + (panel * 128 + 32 * w + n) * 8);
  asm volatile("s_waitcnt vmcnt(0)" ::: "memory");
  __syncthreads();
  const int a = (l >> 2) & 3, p = l & 3, fh = (l >> 4) & 1;
  LAS char* wb[4];
#pragma unroll
  for (int T = 0; T < 4; ++T) wb[T] = sW + (8 * hh + a) * 256 + (((4 * T + 2 * fh + (p >> 1)) ^ (a << 2)) * 16) + 8 * (p & 1);
  {
    f16v c = {};
    LAS char* wt = sW + (8 * hh + a) * 256 + (((4 * w + 2 * fh + (p >> 1)) ^ (a << 2)) * 16) + 8 * (p & 1);
    const v4u ones = {0x3f803f80u, 0x3f803f80u, 0x3f803f80u, 0x3f803f80u};
#pragma unroll
    for (int s = 0; s < 32; ++s) {
      const v2u lo = trrd(wt + s * 4096), hi = trrd(wt + s * 4096 + 1024);
      c = mfma32(v4u{lo.x, lo.y, hi.x, hi.y}, ones, c);
    }
    if (n == 0) {
#pragma unroll
      for (int g = 0; g < 4; ++g) *(LAS f4*)(sC + (w * 32 + 8 * g + 4 * hh) * 4) = f4{c[4 * g], c[4 * g + 1], c[4 * g + 2], c[4 * g + 3]};
    }
  }
  __syncthreads();
  f16v acc[4] = {};
#pragma unroll
  for (int s = 0; s < 32; ++s)
#pragma unroll
    for (int T = 0; T < 4; ++T) {
      const v2u lo = trrd(wb[T] + s * 4096), hi = trrd(wb[T] + s * 4096 + 1024);
      acc[T] = mfma32(v4u{lo.x, lo.y, hi.x, hi.y}, hf[s], acc[T]);
    }
  const int g32 = 4 * panel + w;
  char* op = ws + OFF_HH + ((g32 >> 1) * 112 + slice * 8) * 2048 + (g32 & 1) * 1024 + l * 16;
#pragma unroll
  for (int T = 0; T < 4; ++T)
#pragma unroll
    for (int s = 0; s < 2; ++s) {
      v4u o;
#pragma unroll
      for (int g = 0; g < 2; ++g) {
        const f4 c = *(LAS f4*)(sC + (T * 32 + 8 * (2 * s + g) + 4 * hh) * 4);
#pragma unroll
        for (int e = 0; e < 2; ++e) {
          const int rg = 8 * s + 4 * g + 2 * e;
          o[2 * g + e] = pk(fmaxf(acc[T][rg] - st.x * c[2 * e], 0.f), fmaxf(acc[T][rg + 1] - st.x * c[2 * e + 1], 0.f));
        }
      }
      *(v4u*)(op + (2 * T + s) * 2048) = o;
    }
}

__global__ void __launch_bounds__(256) k_g2(const char* __restrict__ w2, const char* __restrict__ ws, char* __restrict__ out) {
  extern __shared__ __attribute__((aligned(16))) char dsm_[];
  LAS char* sW = (LAS char*)dsm_;
  const int b = blockIdx.x, x = b & 7, i = b >> 3, t = threadIdx.x, l = t & 63, w = __builtin_amdgcn_readfirstlane(t >> 6);
  const int tt = 4 * x + (i & 3), cs = i >> 2;
  const int n = l & 31, hh = l >> 5, a = (l >> 2) & 3, p = l & 3, fh = (l >> 4) & 1;
  f16v acc[2][2] = {};
  LAS char* mw = sW + w * 28672;
  LAS char* wb[2];
#pragma unroll
  for (int T = 0; T < 2; ++T) wb[T] = mw + (4 * hh + a) * 128 + (((4 * T + 2 * fh + (p >> 1)) ^ (((a >> 1) & 1) << 2)) * 16) + 8 * (p & 1);
  for (int half = 0; half < 2; ++half) {
    const int ks0 = 28 * w + 14 * half;
#pragma unroll
    for (int m = 0; m < 28; ++m) {
      const int row = ks0 * 16 + m * 8 + (l >> 3);
      dma16(w2 + (size_t)row * 1024 + cs * 128 + ((l & 7) ^ (((l >> 4) & 1) << 2)) * 16, mw + m * 1024);
    }
    v4u hf[14][2];
#pragma unroll
    for (int s = 0; s < 14; ++s)
#pragma unroll
      for (int th = 0; th < 2; ++th) hf[s][th] = *(const v4u*)(ws + OFF_HH + (tt * 112 + ks0 + s) * 2048 + th * 1024 + l * 16);
    asm volatile("s_waitcnt vmcnt(0)" ::: "memory");
#pragma unroll
    for (int s = 0; s < 14; ++s)
#pragma unroll
      for (int T = 0; T < 2; ++T) {
        const v2u lo = trrd(wb[T] + s * 2048), hi = trrd(wb[T] + s * 2048 + 1024);
        const v4u wf = {lo.x, lo.y, hi.x, hi.y};
#pragma unroll
        for (int th = 0; th < 2; ++th) acc[th][T] = mfma32(wf, hf[s][th], acc[th][T]);
      }
    asm volatile("s_waitcnt lgkmcnt(0)" ::: "memory");
  }
  __syncthreads();
#pragma unroll
  for (int th = 0; th < 2; ++th)
#pragma unroll
    for (int T = 0; T < 2; ++T)
#pragma unroll
      for (int g = 0; g < 4; ++g)
        *(LAS f4*)(sW + (w * 64 + th * 32 + n) * 272 + (T * 32 + 8 * g + 4 * hh) * 4) =
            f4{acc[th][T][4 * g], acc[th][T][4 * g + 1], acc[th][T][4 * g + 2], acc[th][T][4 * g + 3]};
  __syncthreads();
#pragma unroll
  for (int j = 0; j < 2; ++j) {
    const int pl = (t >> 3) + 32 * j, c8 = t & 7, pos = tt * 64 + pl;
    f4 s0 = {0, 0, 0, 0}, s1 = {0, 0, 0, 0};
#pragma unroll
    for (int ww = 0; ww < 4; ++ww) { s0 += *(LAS f4*)(sW + (ww * 64 + pl) * 272 + c8 * 32); s1 += *(LAS f4*)(sW + (ww * 64 + pl) * 272 + c8 * 32 + 16); }
    const float rs = *(const float*)(ws + OFF_ST2 + pos * 8 + 4);
    unsigned hr[4];
#pragma unroll
    for (int e = 0; e < 4; ++e) hr[e] = *(const unsigned*)(ws + OFF_HB + (pos >> 7) * 131072 + (cs * 32 + c8 * 4 + e) * 512 + (pos & 127) * 4);
    v4u o = {pk(bflo(hr[0]) + rs * s0[0], bfhi(hr[0]) + rs * s0[1]), pk(bflo(hr[1]) + rs * s0[2], bfhi(hr[1]) + rs * s0[3]),
             pk(bflo(hr[2]) + rs * s1[0], bfhi(hr[2]) + rs * s1[1]), pk(bflo(hr[3]) + rs * s1[2], bfhi(hr[3]) + rs * s1[3])};
    *(v4u*)(out + (size_t)pos2tok(pos) * 1024 + cs * 128 + c8 * 16) = o;
  }
}


__global__ void k_amp(const char* ws) {
  __shared__ unsigned long long a[256], z[256];
  const unsigned long long* sp = (const unsigned long long*)(ws + (128u << 20));
  a[threadIdx.x] = sp[threadIdx.x * 2]; z[threadIdx.x] = sp[threadIdx.x * 2 + 1];
  __syncthreads();
  if (threadIdx.x == 0) {
    unsigned long long sum = 0, mx = 0, smin = ~0ull, smax = 0, emax = 0;
    for (int j = 0; j < 256; ++j) { unsigned long long d = z[j] - a[j]; sum += d; mx = d > mx ? d : mx; smin = a[j] < smin ? a[j] : smin; smax = a[j] > smax ? a[j] : smax; emax = z[j] > emax ? z[j] : emax; }
    unsigned long long v = MODE == 0 ? 0 : MODE == 1 ? sum * 50 / 256 : MODE == 2 ? mx * 50 : MODE == 3 ? (smax - smin) * 50 : (emax - smax) * 50;
    if (v > 100000) v = 100000;
    const unsigned long long t1 = __builtin_amdgcn_s_memrealtime();
    while (__builtin_amdgcn_s_memrealtime() - t1 < v) {}
  }
}
extern "C" void kernel_launch(void* const* d_in, const int* in_sizes, int n_in, void* d_out, int out_size, void* d_ws, size_t ws_size,
                              hipStream_t stream) {
  const char* q = (const char*)d_in[0]; const char* k = (const char*)d_in[1]; const char* v = (const char*)d_in[2];
  const char* sk = (const char*)d_in[3]; const char* g = (const char*)d_in[4]; const unsigned short* Dv = (const unsigned short*)d_in[5];
  const char* w1 = (const char*)d_in[7]; const char* w2 = (const char*)d_in[8];
  char* ws = (char*)d_ws;
  (void)hipFuncSetAttribute((const void*)k_g1, hipFuncAttributeMaxDynamicSharedMemorySize, 131072 + 512);
  (void)hipFuncSetAttribute((const void*)k_g2, hipFuncAttributeMaxDynamicSharedMemorySize, 114688);
  k_p0<<<256, 512, 0, stream>>>(q, k, v, sk, ws);
  k_conv<<<256, 256, 0, stream>>>(g, Dv, ws);
  k_st2<<<32, 256, 0, stream>>>(ws);
  k_g1<<<256, 256, 131072 + 512, stream>>>(w1, ws);
  k_g2<<<256, 256, 114688, stream>>>(w2, ws, (char*)d_out);
  k_amp<<<1, 256, 0, stream>>>(ws);
}
```

```cpp
#include <hip/hip_runtime.h>

#define SRD(n) const void* p##n, unsigned n##_bytes, unsigned n##_fmt
__global__ void __launch_bounds__(512, 2)
h3_ffn_mega(SRD(k), SRD(v), SRD(sk), SRD(ws), SRD(q), SRD(g), SRD(dv), SRD(w2), SRD(w1), SRD(out)) {
  asm volatile(
    ".Lh3_KSTART:\n"
    "s_load_dwordx16 s[16:31], s[0:1], 0\n"
    "s_load_dwordx16 s[32:47], s[0:1], 64\n"
    "s_load_dwordx8 s[48:55], s[0:1], 128\n"
    "v_readfirstlane_b32 s3, v0\n"
    "s_and_b32 s4, s2, 7\n"
    "s_lshr_b32 s5, s2, 3\n"
    "s_lshr_b32 s56, s2, 6\n"
    "s_lshl2_add_u32 s6, s4, s56\n"
    "s_and_b32 s7, s5, 7\n"
    "s_xor_b32 s7, s7, 0\n"
    "s_bfe_u32 s13, s3, 0x20006\n"
    "v_and_b32 v1, 63, v0\n"
    "s_cmp_lt_u32 s3, 256\n"
    "s_cbranch_scc1 .Lh3_TEAMA\n"
    "s_branch .Lh3_TEAMB\n"
    ".Lh3_TEAMA:\n"
    "s_setprio 2\n"
    "v_and_b32 v3, 7, v0\n"
    "v_lshlrev_b32 v3, 4, v3\n"
    "v_lshrrev_b32 v2, 3, v1\n"
    "v_lshl_or_b32 v2, v2, 10, v3\n"
    "s_lshl_b32 s56, s6, 16\n"
    "s_lshl_b32 s57, s13, 13\n"
    "s_add_u32 s56, s56, s57\n"
    "s_lshl_b32 s57, s7, 7\n"
    "s_add_u32 s56, s56, s57\n"
    "s_mul_i32 s57, s13, 1040\n"
    "s_add_u32 s58, s56, 32768\n"
    "s_add_u32 m0, s57, 132112\n"
    "v_lshrrev_b32 v5, 3, v0\n"
    "v_lshl_or_b32 v4, v5, 13, v3\n"
    "s_lshl_b32 s60, s7, 18\n"
    "s_lshl_b32 s61, s6, 7\n"
    "s_add_u32 s60, s60, s61\n"
    "s_add_u32 s60, s60, 65536\n"
    "s_add_u32 s61, s60, 4096\n"
    "v_mov_b32 v16, -1\n"
    "v_mov_b32 v17, -1\n"
    "v_mov_b32 v18, -1\n"
    "v_mov_b32 v19, -1\n"
    "s_waitcnt lgkmcnt(0)\n"
    "buffer_load_dwordx4 v2, s[16:19], s56 offen nt lds\n"
    "s_add_u32 m0, s57, 140432\n"
    "v_lshlrev_b32 v6, 4, v5\n"
    "buffer_load_dwordx4 v2, s[20:23], s56 offen nt lds\n"
    "s_add_u32 m0, s57, 136272\n"
    "s_lshl_b32 s59, s7, 9\n"
    "buffer_load_dwordx4 v2, s[16:19], s58 offen nt lds\n"
    "s_add_u32 m0, s57, 144592\n"
    "s_cmp_lg_u32 s13, 0\n"
    "buffer_load_dwordx4 v2, s[20:23], s58 offen nt lds\n"
    "s_cbranch_scc1 .Lh3_A_nohalo\n"
    "s_sub_u32 s58, s56, 8192\n"
    "s_cmp_eq_u32 s6, 0\n"
    "s_cselect_b32 s58, 0, s58\n"
    "s_cselect_b32 s62, 2147483648, 0\n"
    "s_mov_b32 m0, 131072\n"
    "v_add_u32 v7, s62, v2\n"
    "s_mov_b32 exec_lo, 0\n"
    "s_mov_b32 exec_hi, 0xffffff00\n"
    "buffer_load_dwordx4 v7, s[16:19], s58 offen nt lds\n"
    "s_mov_b64 exec, -1\n"
    ".Lh3_A_nohalo:\n"
    "buffer_load_dwordx4 v[8:11], v6, s[24:27], s59 offen\n"
    "buffer_load_dwordx2 v[12:13], off, s[28:31], 0 sc1\n"
    "buffer_store_dwordx4 v[16:19], v4, s[28:31], s60 offen sc1\n"
    "buffer_store_dwordx4 v[16:19], v4, s[28:31], s61 offen sc1\n"
    "s_barrier\n"
    "s_lshl_b32 s8, s4, 5\n"
    "s_add_u32 s8, s8, s5\n"
    "s_and_b32 s56, s5, 3\n"
    "s_lshr_b32 s9, s4, 1\n"
    "s_lshl2_add_u32 s9, s9, s56\n"
    "s_lshl2_add_u32 s11, s4, s56\n"
    "s_lshr_b32 s12, s5, 2\n"
    "s_and_b32 s10, s4, 1\n"
    "s_mul_i32 s10, s10, 7\n"
    "s_add_u32 s10, s10, s12\n"
    "s_movk_i32 s14, 0x4000\n"
    "s_mov_b32 s3, 0x100000\n"
    "s_mov_b32 s15, 0xffff0000\n"
    "v_and_b32 v14, 7, v0\n"
    "v_mul_u32_u24 v14, 1040, v14\n"
    "v_lshl_add_u32 v14, v5, 2, v14\n"
    "v_add_u32 v112, 140432, v14\n"
    "v_add_u32 v15, 132112, v14\n"
    "v_add_u32 v14, 131072, v14\n"
    "v_mov_b32 v113, 118848\n"
    "v_mov_b32 v114, 1\n"
    "v_and_b32 v115, 15, v1\n"
    "v_lshrrev_b32 v125, 4, v1\n"
    "v_add_u32 v116, 9, v115\n"
    "v_lshrrev_b32 v116, 1, v116\n"
    "v_lshlrev_b32 v117, 2, v125\n"
    "v_sub_u32 v116, v116, v117\n"
    "s_and_b32 s56, s13, 1\n"
    "s_lshl_b32 s57, s56, 8\n"
    "s_cmp_ge_u32 s13, 2\n"
    "s_mov_b32 s58, 114720\n"
    "s_cselect_b32 s58, 157104, s58\n"
    "s_mov_b32 s59, 122144\n"
    "s_cselect_b32 s59, 126752, s59\n"
    "s_add_u32 s58, s58, s57\n"
    "s_sub_u32 s59, s59, s57\n"
    "v_lshl_add_u32 v116, v116, 2, s58\n"
    "v_add_u32 v117, 1024, v116\n"
    "v_add_u32 v118, 2048, v116\n"
    "v_add_u32 v119, 3072, v116\n"
    "v_and_b32 v120, 1, v1\n"
    "v_cmp_eq_u32 vcc, 1, v120\n"
    "v_mov_b32 v120, 0x03020504\n"
    "v_mov_b32 v121, 0x01000302\n"
    "v_cndmask_b32 v120, v120, v121, vcc\n"
    "v_and_b32 v121, 7, v1\n"
    "v_lshlrev_b32 v121, 7, v121\n"
    "v_lshlrev_b32 v6, 4, v0\n"
    "v_add_u32 v7, 122368, v6\n"
    "v_lshl_add_u32 v122, v115, 1, v125\n"
    "v_lshl_add_u32 v122, v122, 4, s59\n"
    "s_lshl_b32 s57, s13, 13\n"
    "s_add_u32 s57, s57, 131072\n"
    "v_and_b32 v126, 1, v125\n"
    "v_xor_b32 v126, v126, v1\n"
    "v_lshl_add_u32 v123, v126, 4, s57\n"
    "s_lshl2_add_u32 s62, s6, s13\n"
    "s_lshl_b32 s62, s62, 10\n"
    "s_add_u32 s62, s62, 13729792\n"
    "s_lshl2_add_u32 s67, s7, s13\n"
    "s_lshl_b32 s67, s67, 5\n"
    "s_add_u32 s67, s67, s6\n"
    "s_lshl_b32 s67, s67, 7\n"
    "s_add_u32 s67, s67, 13729792\n"
    "s_lshl_b32 s63, s8, 13\n"
    "s_add_u32 s63, s63, 65536\n"
    "s_add_u32 s66, s63, 4096\n"
    "s_waitcnt vmcnt(3)\n"
    "s_mov_b64 exec, 1\n"
    "ds_add_u32 v113, v114 offset:0\n"
    "s_mov_b64 exec, -1\n"
    "v_lshlrev_b32 v78, 16, v8\n"
    "v_lshlrev_b32 v79, 16, v10\n"
    "v_and_b32 v80, s15, v8\n"
    "v_and_b32 v81, s15, v10\n"
    "v_lshlrev_b32 v82, 16, v9\n"
    "v_lshlrev_b32 v83, 16, v11\n"
    "v_and_b32 v84, s15, v9\n"
    "v_and_b32 v85, s15, v11\n"
    ".Lh3_w_1:\n"
    "ds_read_b32 v20, v113 offset:0\n"
    "s_cmp_eq_u32 s3, 0\n"
    "s_cbranch_scc1 .Lh3_w_1x\n"
    "s_sub_u32 s3, s3, 1\n"
    "s_waitcnt lgkmcnt(0)\n"
    "v_cmp_gt_u32 vcc, 4, v20\n"
    "s_cbranch_vccnz .Lh3_w_1\n"
    ".Lh3_w_1x:\n"
    "ds_read2_b32 v[20:21], v14 offset0:160 offset1:192\n"
    "ds_read_b32 v22, v14 offset:896\n"
    "ds_read2_b32 v[24:25], v15 offset0:0 offset1:32\n"
    "ds_read2_b32 v[26:27], v15 offset0:64 offset1:96\n"
    "ds_read2_b32 v[28:29], v15 offset0:128 offset1:160\n"
    "ds_read2_b32 v[30:31], v15 offset0:192 offset1:224\n"
    "ds_read2_b32 v[32:33], v112 offset0:0 offset1:32\n"
    "ds_read2_b32 v[34:35], v112 offset0:64 offset1:96\n"
    "ds_read2_b32 v[36:37], v112 offset0:128 offset1:160\n"
    "ds_read2_b32 v[38:39], v112 offset0:192 offset1:224\n"
    "s_waitcnt lgkmcnt(8)\n"
    "v_lshlrev_b32 v40, 16, v20\n"
    "v_and_b32 v41, s15, v20\n"
    "v_lshlrev_b32 v42, 16, v21\n"
    "v_and_b32 v43, s15, v21\n"
    "v_lshlrev_b32 v44, 16, v22\n"
    "v_and_b32 v45, s15, v22\n"
    "s_waitcnt lgkmcnt(7)\n"
    "v_lshlrev_b32 v46, 16, v24\n"
    "v_and_b32 v47, s15, v24\n"
    "v_lshlrev_b32 v48, 16, v25\n"
    "v_and_b32 v49, s15, v25\n"
    "s_waitcnt lgkmcnt(6)\n"
    "v_lshlrev_b32 v50, 16, v26\n"
    "v_and_b32 v51, s15, v26\n"
    "v_lshlrev_b32 v52, 16, v27\n"
    "v_and_b32 v53, s15, v27\n"
    "s_waitcnt lgkmcnt(5)\n"
    "v_lshlrev_b32 v54, 16, v28\n"
    "v_and_b32 v55, s15, v28\n"
    "v_lshlrev_b32 v56, 16, v29\n"
    "v_and_b32 v57, s15, v29\n"
    "s_waitcnt lgkmcnt(4)\n"
    "v_lshlrev_b32 v58, 16, v30\n"
    "v_and_b32 v59, s15, v30\n"
    "v_lshlrev_b32 v60, 16, v31\n"
    "v_and_b32 v61, s15, v31\n"
    "v_pk_mul_f32 v[86:87], v[40:41], v[84:85]\n"
    "v_pk_mul_f32 v[88:89], v[42:43], v[84:85]\n"
    "v_pk_mul_f32 v[90:91], v[44:45], v[84:85]\n"
    "v_pk_mul_f32 v[92:93], v[46:47], v[84:85]\n"
    "v_pk_mul_f32 v[94:95], v[48:49], v[84:85]\n"
    "v_pk_mul_f32 v[96:97], v[50:51], v[84:85]\n"
    "v_pk_mul_f32 v[98:99], v[52:53], v[84:85]\n"
    "v_pk_mul_f32 v[100:101], v[54:55], v[84:85]\n"
    "v_pk_fma_f32 v[86:87], v[42:43], v[82:83], v[86:87]\n"
    "v_pk_fma_f32 v[88:89], v[44:45], v[82:83], v[88:89]\n"
    "v_pk_fma_f32 v[90:91], v[46:47], v[82:83], v[90:91]\n"
    "v_pk_fma_f32 v[92:93], v[48:49], v[82:83], v[92:93]\n"
    "v_pk_fma_f32 v[94:95], v[50:51], v[82:83], v[94:95]\n"
    "v_pk_fma_f32 v[96:97], v[52:53], v[82:83], v[96:97]\n"
    "v_pk_fma_f32 v[98:99], v[54:55], v[82:83], v[98:99]\n"
    "v_pk_fma_f32 v[100:101], v[56:57], v[82:83], v[100:101]\n"
    "v_pk_fma_f32 v[86:87], v[44:45], v[80:81], v[86:87]\n"
    "v_pk_fma_f32 v[88:89], v[46:47], v[80:81], v[88:89]\n"
    "v_pk_fma_f32 v[90:91], v[48:49], v[80:81], v[90:91]\n"
    "v_pk_fma_f32 v[92:93], v[50:51], v[80:81], v[92:93]\n"
    "v_pk_fma_f32 v[94:95], v[52:53], v[80:81], v[94:95]\n"
    "v_pk_fma_f32 v[96:97], v[54:55], v[80:81], v[96:97]\n"
    "v_pk_fma_f32 v[98:99], v[56:57], v[80:81], v[98:99]\n"
    "v_pk_fma_f32 v[100:101], v[58:59], v[80:81], v[100:101]\n"
    "v_pk_fma_f32 v[86:87], v[46:47], v[78:79], v[86:87]\n"
    "v_pk_fma_f32 v[88:89], v[48:49], v[78:79], v[88:89]\n"
    "v_pk_fma_f32 v[90:91], v[50:51], v[78:79], v[90:91]\n"
    "v_pk_fma_f32 v[92:93], v[52:53], v[78:79], v[92:93]\n"
    "v_pk_fma_f32 v[94:95], v[54:55], v[78:79], v[94:95]\n"
    "v_pk_fma_f32 v[96:97], v[56:57], v[78:79], v[96:97]\n"
    "v_pk_fma_f32 v[98:99], v[58:59], v[78:79], v[98:99]\n"
    "v_pk_fma_f32 v[100:101], v[60:61], v[78:79], v[100:101]\n"
    "s_waitcnt vmcnt(0)\n"
    "v_readfirstlane_b32 s64, v12\n"
    "v_readfirstlane_b32 s65, v13\n"
    "s_mul_i32 s64, s64, 1664525\n"
    "s_mul_i32 s65, s65, 22695477\n"
    "s_add_u32 s64, s64, 1013904223\n"
    "s_add_u32 s65, s65, 0x3c6ef35f\n"
    "v_mov_b32 v12, s64\n"
    "v_mov_b32 v13, s65\n"
    "s_waitcnt lgkmcnt(0)\n"
    "v_lshlrev_b32 v62, 16, v32\n"
    "v_and_b32 v63, s15, v32\n"
    "v_lshlrev_b32 v64, 16, v33\n"
    "v_and_b32 v65, s15, v33\n"
    "v_lshlrev_b32 v66, 16, v34\n"
    "v_and_b32 v67, s15, v34\n"
    "v_lshlrev_b32 v68, 16, v35\n"
    "v_and_b32 v69, s15, v35\n"
    "v_lshlrev_b32 v70, 16, v36\n"
    "v_and_b32 v71, s15, v36\n"
    "v_lshlrev_b32 v72, 16, v37\n"
    "v_and_b32 v73, s15, v37\n"
    "v_lshlrev_b32 v74, 16, v38\n"
    "v_and_b32 v75, s15, v38\n"
    "v_lshlrev_b32 v76, 16, v39\n"
    "v_and_b32 v77, s15, v39\n"
    "v_pk_mul_f32 v[86:87], v[86:87], v[62:63]\n"
    "v_pk_mul_f32 v[88:89], v[88:89], v[64:65]\n"
    "v_pk_mul_f32 v[90:91], v[90:91], v[66:67]\n"
    "v_pk_mul_f32 v[92:93], v[92:93], v[68:69]\n"
    "v_pk_mul_f32 v[94:95], v[94:95], v[70:71]\n"
    "v_pk_mul_f32 v[96:97], v[96:97], v[72:73]\n"
    "v_pk_mul_f32 v[98:99], v[98:99], v[74:75]\n"
    "v_pk_mul_f32 v[100:101], v[100:101], v[76:77]\n"
    "v_cvt_pk_bf16_f32 v104, v86, v88\n"
    "v_cvt_pk_bf16_f32 v105, v90, v92\n"
    "v_cvt_pk_bf16_f32 v106, v94, v96\n"
    "v_cvt_pk_bf16_f32 v107, v98, v100\n"
    "buffer_store_dwordx4 v[104:107], v4, s[28:31], s60 offen sc1\n"
    "v_cvt_pk_bf16_f32 v108, v87, v89\n"
    "v_cvt_pk_bf16_f32 v109, v91, v93\n"
    "v_cvt_pk_bf16_f32 v110, v95, v97\n"
    "v_cvt_pk_bf16_f32 v111, v99, v101\n"
    "buffer_store_dwordx4 v[108:111], v4, s[28:31], s61 offen sc1\n"
    "s_mov_b64 exec, 1\n"
    "buffer_store_dwordx2 v[12:13], off, s[28:31], s67 sc1\n"
    "s_mov_b64 exec, -1\n"
    ".Lh3_w_2:\n"
    "ds_read_b32 v20, v113 offset:4\n"
    "s_cmp_eq_u32 s3, 0\n"
    "s_cbranch_scc1 .Lh3_w_2x\n"
    "s_sub_u32 s3, s3, 1\n"
    "s_waitcnt lgkmcnt(0)\n"
    "v_cmp_gt_u32 vcc, 2, v20\n"
    "s_cbranch_vccnz .Lh3_w_2\n"
    ".Lh3_w_2x:\n"
    "ds_read2_b32 v[20:21], v116 offset0:0 offset1:1\n"
    "ds_read2_b32 v[22:23], v116 offset0:2 offset1:3\n"
    "ds_read_b32 v24, v116 offset:16\n"
    "ds_read2_b32 v[26:27], v116 offset0:128 offset1:129\n"
    "ds_read2_b32 v[28:29], v116 offset0:130 offset1:131\n"
    "ds_read_b32 v30, v116 offset:528\n"
    "ds_read2_b32 v[32:33], v117 offset0:0 offset1:1\n"
    "ds_read2_b32 v[34:35], v117 offset0:2 offset1:3\n"
    "ds_read_b32 v36, v117 offset:16\n"
    "ds_read2_b32 v[38:39], v117 offset0:128 offset1:129\n"
    "ds_read2_b32 v[40:41], v117 offset0:130 offset1:131\n"
    "ds_read_b32 v42, v117 offset:528\n"
    "s_waitcnt lgkmcnt(9)\n"
    "v_perm_b32 v128, v24, v23, v120\n"
    "v_perm_b32 v129, v23, v22, v120\n"
    "v_perm_b32 v130, v22, v21, v120\n"
    "v_perm_b32 v131, v21, v20, v120\n"
    "ds_read2_b32 v[20:21], v118 offset0:0 offset1:1\n"
    "ds_read2_b32 v[22:23], v118 offset0:2 offset1:3\n"
    "ds_read_b32 v24, v118 offset:16\n"
    "s_waitcnt lgkmcnt(9)\n"
    "v_perm_b32 v132, v30, v29, v120\n"
    "v_perm_b32 v133, v29, v28, v120\n"
    "v_perm_b32 v134, v28, v27, v120\n"
    "v_perm_b32 v135, v27, v26, v120\n"
    "ds_read2_b32 v[26:27], v118 offset0:128 offset1:129\n"
    "ds_read2_b32 v[28:29], v118 offset0:130 offset1:131\n"
    "ds_read_b32 v30, v118 offset:528\n"
    "s_waitcnt lgkmcnt(9)\n"
    "v_perm_b32 v136, v36, v35, v120\n"
    "v_perm_b32 v137, v35, v34, v120\n"
    "v_perm_b32 v138, v34, v33, v120\n"
    "v_perm_b32 v139, v33, v32, v120\n"
    "ds_read2_b32 v[32:33], v119 offset0:0 offset1:1\n"
    "ds_read2_b32 v[34:35], v119 offset0:2 offset1:3\n"
    "ds_read_b32 v36, v119 offset:16\n"
    "s_waitcnt lgkmcnt(9)\n"
    "v_perm_b32 v140, v42, v41, v120\n"
    "v_perm_b32 v141, v41, v40, v120\n"
    "v_perm_b32 v142, v40, v39, v120\n"
    "v_perm_b32 v143, v39, v38, v120\n"
    "ds_read2_b32 v[38:39], v119 offset0:128 offset1:129\n"
    "ds_read2_b32 v[40:41], v119 offset0:130 offset1:131\n"
    "ds_read_b32 v42, v119 offset:528\n"
    "s_waitcnt lgkmcnt(9)\n"
    "v_perm_b32 v144, v24, v23, v120\n"
    "v_perm_b32 v145, v23, v22, v120\n"
    "v_perm_b32 v146, v22, v21, v120\n"
    "v_perm_b32 v147, v21, v20, v120\n"
    "ds_read2_b32 v[20:21], v116 offset0:16 offset1:17\n"
    "ds_read2_b32 v[22:23], v116 offset0:18 offset1:19\n"
    "ds_read_b32 v24, v116 offset:80\n"
    "s_waitcnt lgkmcnt(9)\n"
    "v_perm_b32 v148, v30, v29, v120\n"
    "v_perm_b32 v149, v29, v28, v120\n"
    "v_perm_b32 v150, v28, v27, v120\n"
    "v_perm_b32 v151, v27, v26, v120\n"
    "ds_read2_b32 v[26:27], v116 offset0:144 offset1:145\n"
    "ds_read2_b32 v[28:29], v116 offset0:146 offset1:147\n"
    "ds_read_b32 v30, v116 offset:592\n"
    "s_waitcnt lgkmcnt(9)\n"
    "v_perm_b32 v152, v36, v35, v120\n"
    "v_perm_b32 v153, v35, v34, v120\n"
    "v_perm_b32 v154, v34, v33, v120\n"
    "v_perm_b32 v155, v33, v32, v120\n"
    "ds_read2_b32 v[32:33], v117 offset0:16 offset1:17\n"
    "ds_read2_b32 v[34:35], v117 offset0:18 offset1:19\n"
    "ds_read_b32 v36, v117 offset:80\n"
    "s_waitcnt lgkmcnt(9)\n"
    "v_perm_b32 v156, v42, v41, v120\n"
    "v_perm_b32 v157, v41, v40, v120\n"
    "v_perm_b32 v158, v40, v39, v120\n"
    "v_perm_b32 v159, v39, v38, v120\n"
    "ds_read2_b32 v[38:39], v117 offset0:144 offset1:145\n"
    "ds_read2_b32 v[40:41], v117 offset0:146 offset1:147\n"
    "ds_read_b32 v42, v117 offset:592\n"
    "s_waitcnt lgkmcnt(9)\n"
    "v_perm_b32 v160, v24, v23, v120\n"
    "v_perm_b32 v161, v23, v22, v120\n"
    "v_perm_b32 v162, v22, v21, v120\n"
    "v_perm_b32 v163, v21, v20, v120\n"
    "ds_read2_b32 v[20:21], v118 offset0:16 offset1:17\n"
    "ds_read2_b32 v[22:23], v118 offset0:18 offset1:19\n"
    "ds_read_b32 v24, v118 offset:80\n"
    "s_waitcnt lgkmcnt(9)\n"
    "v_perm_b32 v164, v30, v29, v120\n"
    "v_perm_b32 v165, v29, v28, v120\n"
    "v_perm_b32 v166, v28, v27, v120\n"
    "v_perm_b32 v167, v27, v26, v120\n"
    "ds_read2_b32 v[26:27], v118 offset0:144 offset1:145\n"
    "ds_read2_b32 v[28:29], v118 offset0:146 offset1:147\n"
    "ds_read_b32 v30, v118 offset:592\n"
    "s_waitcnt lgkmcnt(9)\n"
    "v_perm_b32 v168, v36, v35, v120\n"
    "v_perm_b32 v169, v35, v34, v120\n"
    "v_perm_b32 v170, v34, v33, v120\n"
    "v_perm_b32 v171, v33, v32, v120\n"
    "ds_read2_b32 v[32:33], v119 offset0:16 offset1:17\n"
    "ds_read2_b32 v[34:35], v119 offset0:18 offset1:19\n"
    "ds_read_b32 v36, v119 offset:80\n"
    "s_waitcnt lgkmcnt(9)\n"
    "v_perm_b32 v172, v42, v41, v120\n"
    "v_perm_b32 v173, v41, v40, v120\n"
    "v_perm_b32 v174, v40, v39, v120\n"
    "v_perm_b32 v175, v39, v38, v120\n"
    "ds_read2_b32 v[38:39], v119 offset0:144 offset1:145\n"
    "ds_read2_b32 v[40:41], v119 offset0:146 offset1:147\n"
    "ds_read_b32 v42, v119 offset:592\n"
    "buffer_load_dwordx2 v[44:45], v121, s[28:31], s62 offen sc1\n"
    "s_waitcnt lgkmcnt(9)\n"
    "v_perm_b32 v176, v24, v23, v120\n"
    "v_perm_b32 v177, v23, v22, v120\n"
    "v_perm_b32 v178, v22, v21, v120\n"
    "v_perm_b32 v179, v21, v20, v120\n"
    "ds_read2_b32 v[20:21], v116 offset0:32 offset1:33\n"
    "ds_read2_b32 v[22:23], v116 offset0:34 offset1:35\n"
    "ds_read_b32 v24, v116 offset:144\n"
    "s_waitcnt lgkmcnt(9)\n"
    "v_perm_b32 v180, v30, v29, v120\n"
    "v_perm_b32 v181, v29, v28, v120\n"
    "v_perm_b32 v182, v28, v27, v120\n"
    "v_perm_b32 v183, v27, v26, v120\n"
    "ds_read2_b32 v[26:27], v116 offset0:160 offset1:161\n"
    "ds_read2_b32 v[28:29], v116 offset0:162 offset1:163\n"
    "ds_read_b32 v30, v116 offset:656\n"
    "s_waitcnt lgkmcnt(9)\n"
    "v_perm_b32 v184, v36, v35, v120\n"
    "v_perm_b32 v185, v35, v34, v120\n"
    "v_perm_b32 v186, v34, v33, v120\n"
    "v_perm_b32 v187, v33, v32, v120\n"
    "ds_read2_b32 v[32:33], v117 offset0:32 offset1:33\n"
    "ds_read2_b32 v[34:35], v117 offset0:34 offset1:35\n"
    "ds_read_b32 v36, v117 offset:144\n"
    "s_waitcnt lgkmcnt(9)\n"
    "v_perm_b32 v188, v42, v41, v120\n"
    "v_perm_b32 v189, v41, v40, v120\n"
    "v_perm_b32 v190, v40, v39, v120\n"
    "v_perm_b32 v191, v39, v38, v120\n"
    "ds_read2_b32 v[38:39], v117 offset0:160 offset1:161\n"
    "ds_read2_b32 v[40:41], v117 offset0:162 offset1:163\n"
    "ds_read_b32 v42, v117 offset:656\n"
    "s_waitcnt lgkmcnt(9)\n"
    "v_perm_b32 v192, v24, v23, v120\n"
    "v_perm_b32 v193, v23, v22, v120\n"
    "v_perm_b32 v194, v22, v21, v120\n"
    "v_perm_b32 v195, v21, v20, v120\n"
    "ds_read2_b32 v[20:21], v118 offset0:32 offset1:33\n"
    "ds_read2_b32 v[22:23], v118 offset0:34 offset1:35\n"
    "ds_read_b32 v24, v118 offset:144\n"
    "s_waitcnt lgkmcnt(9)\n"
    "v_perm_b32 v196, v30, v29, v120\n"
    "v_perm_b32 v197, v29, v28, v120\n"
    "v_perm_b32 v198, v28, v27, v120\n"
    "v_perm_b32 v199, v27, v26, v120\n"
    "ds_read2_b32 v[26:27], v118 offset0:160 offset1:161\n"
    "ds_read2_b32 v[28:29], v118 offset0:162 offset1:163\n"
    "ds_read_b32 v30, v118 offset:656\n"
    "s_waitcnt lgkmcnt(9)\n"
    "v_perm_b32 v200, v36, v35, v120\n"
    "v_perm_b32 v201, v35, v34, v120\n"
    "v_perm_b32 v202, v34, v33, v120\n"
    "v_perm_b32 v203, v33, v32, v120\n"
    "ds_read2_b32 v[32:33], v119 offset0:32 offset1:33\n"
    "ds_read2_b32 v[34:35], v119 offset0:34 offset1:35\n"
    "ds_read_b32 v36, v119 offset:144\n"
    "s_waitcnt lgkmcnt(9)\n"
    "v_perm_b32 v204, v42, v41, v120\n"
    "v_perm_b32 v205, v41, v40, v120\n"
    "v_perm_b32 v206, v40, v39, v120\n"
    "v_perm_b32 v207, v39, v38, v120\n"
    "ds_read2_b32 v[38:39], v119 offset0:160 offset1:161\n"
    "ds_read2_b32 v[40:41], v119 offset0:162 offset1:163\n"
    "ds_read_b32 v42, v119 offset:656\n"
    "s_waitcnt lgkmcnt(9)\n"
    "v_perm_b32 v208, v24, v23, v120\n"
    "v_perm_b32 v209, v23, v22, v120\n"
    "v_perm_b32 v210, v22, v21, v120\n"
    "v_perm_b32 v211, v21, v20, v120\n"
    "ds_read2_b32 v[20:21], v116 offset0:48 offset1:49\n"
    "ds_read2_b32 v[22:23], v116 offset0:50 offset1:51\n"
    "ds_read_b32 v24, v116 offset:208\n"
    "s_waitcnt lgkmcnt(9)\n"
    "v_perm_b32 v212, v30, v29, v120\n"
    "v_perm_b32 v213, v29, v28, v120\n"
    "v_perm_b32 v214, v28, v27, v120\n"
    "v_perm_b32 v215, v27, v26, v120\n"
    "ds_read2_b32 v[26:27], v116 offset0:176 offset1:177\n"
    "ds_read2_b32 v[28:29], v116 offset0:178 offset1:179\n"
    "ds_read_b32 v30, v116 offset:720\n"
    "s_waitcnt vmcnt(0)\n"
    "v_xor_b32 v46, s64, v44\n"
    "v_xor_b32 v47, s65, v45\n"
    "v_or_b32 v46, v46, v47\n"
    "v_cmp_ne_u32 vcc, 0, v46\n"
    "s_cbranch_vccnz .Lh3_slow_3\n"
    ".Lh3_back_4:\n"
    "buffer_load_dwordx4 v[48:51], v6, s[28:31], s63 offen sc1\n"
    "buffer_load_dwordx4 v[52:55], v6, s[28:31], s66 offen sc1\n"
    "s_waitcnt lgkmcnt(9)\n"
    "v_perm_b32 v216, v36, v35, v120\n"
    "v_perm_b32 v217, v35, v34, v120\n"
    "v_perm_b32 v218, v34, v33, v120\n"
    "v_perm_b32 v219, v33, v32, v120\n"
    "ds_read2_b32 v[32:33], v117 offset0:48 offset1:49\n"
    "ds_read2_b32 v[34:35], v117 offset0:50 offset1:51\n"
    "ds_read_b32 v36, v117 offset:208\n"
    "s_waitcnt lgkmcnt(9)\n"
    "v_perm_b32 v220, v42, v41, v120\n"
    "v_perm_b32 v221, v41, v40, v120\n"
    "v_perm_b32 v222, v40, v39, v120\n"
    "v_perm_b32 v223, v39, v38, v120\n"
    "ds_read2_b32 v[38:39], v117 offset0:176 offset1:177\n"
    "ds_read2_b32 v[40:41], v117 offset0:178 offset1:179\n"
    "ds_read_b32 v42, v117 offset:720\n"
    "s_waitcnt lgkmcnt(9)\n"
    "v_perm_b32 v224, v24, v23, v120\n"
    "v_perm_b32 v225, v23, v22, v120\n"
    "v_perm_b32 v226, v22, v21, v120\n"
    "v_perm_b32 v227, v21, v20, v120\n"
    "ds_read2_b32 v[20:21], v118 offset0:48 offset1:49\n"
    "ds_read2_b32 v[22:23], v118 offset0:50 offset1:51\n"
    "ds_read_b32 v24, v118 offset:208\n"
    "s_waitcnt lgkmcnt(9)\n"
    "v_perm_b32 v228, v30, v29, v120\n"
    "v_perm_b32 v229, v29, v28, v120\n"
    "v_perm_b32 v230, v28, v27, v120\n"
    "v_perm_b32 v231, v27, v26, v120\n"
    "ds_read2_b32 v[26:27], v118 offset0:176 offset1:177\n"
    "ds_read2_b32 v[28:29], v118 offset0:178 offset1:179\n"
    "ds_read_b32 v30, v118 offset:720\n"
    "s_waitcnt lgkmcnt(9)\n"
    "v_perm_b32 v232, v36, v35, v120\n"
    "v_perm_b32 v233, v35, v34, v120\n"
    "v_perm_b32 v234, v34, v33, v120\n"
    "v_perm_b32 v235, v33, v32, v120\n"
    "ds_read2_b32 v[32:33], v119 offset0:48 offset1:49\n"
    "ds_read2_b32 v[34:35], v119 offset0:50 offset1:51\n"
    "ds_read_b32 v36, v119 offset:208\n"
    "s_waitcnt lgkmcnt(9)\n"
    "v_perm_b32 v236, v42, v41, v120\n"
    "v_perm_b32 v237, v41, v40, v120\n"
    "v_perm_b32 v238, v40, v39, v120\n"
    "v_perm_b32 v239, v39, v38, v120\n"
    "ds_read2_b32 v[38:39], v119 offset0:176 offset1:177\n"
    "ds_read2_b32 v[40:41], v119 offset0:178 offset1:179\n"
    "ds_read_b32 v42, v119 offset:720\n"
    "s_waitcnt lgkmcnt(9)\n"
    "v_perm_b32 v240, v24, v23, v120\n"
    "v_perm_b32 v241, v23, v22, v120\n"
    "v_perm_b32 v242, v22, v21, v120\n"
    "v_perm_b32 v243, v21, v20, v120\n"
    "s_waitcnt lgkmcnt(6)\n"
    "v_perm_b32 v244, v30, v29, v120\n"
    "v_perm_b32 v245, v29, v28, v120\n"
    "v_perm_b32 v246, v28, v27, v120\n"
    "v_perm_b32 v247, v27, v26, v120\n"
    "s_waitcnt lgkmcnt(3)\n"
    "v_perm_b32 v248, v36, v35, v120\n"
    "v_perm_b32 v249, v35, v34, v120\n"
    "v_perm_b32 v250, v34, v33, v120\n"
    "v_perm_b32 v251, v33, v32, v120\n"
    "s_waitcnt lgkmcnt(0)\n"
    "v_perm_b32 v252, v42, v41, v120\n"
    "v_perm_b32 v253, v41, v40, v120\n"
    "v_perm_b32 v254, v40, v39, v120\n"
    "v_perm_b32 v255, v39, v38, v120\n"
    "s_waitcnt vmcnt(0)\n"
    "v_max3_u32 v46, v48, v49, v50\n"
    "v_max3_u32 v46, v46, v51, v52\n"
    "v_max3_u32 v46, v46, v53, v54\n"
    "v_max_u32 v46, v46, v55\n"
    "v_cmp_eq_u32 vcc, -1, v46\n"
    "s_cbranch_vccnz .Lh3_slow_5\n"
    ".Lh3_back_6:\n"
    "ds_write_b128 v7, v[48:51]\n"
    "ds_write_b128 v7, v[52:55] offset:4608\n"
    "s_mov_b64 exec, 1\n"
    "ds_add_u32 v113, v114 offset:8\n"
    "s_mov_b64 exec, -1\n"
    "v_lshrrev_b32 v4, 4, v1\n"
    "v_and_b32 v5, 15, v1\n"
    "v_lshlrev_b32 v2, 1, v4\n"
    "v_xor_b32 v2, v2, v5\n"
    "v_lshlrev_b32 v2, 4, v2\n"
    "v_mul_u32_u24 v4, 3584, v4\n"
    "v_xor_b32 v3, 0x80, v2\n"
    "v_add_u32 v2, v2, v4\n"
    "v_add_u32 v3, v3, v4\n"
    "s_lshr_b32 s56, s5, 2\n"
    "s_mul_i32 s56, s56, 0\n"
    "s_lshr_b32 s56, s56, 4\n"
    "s_lshr_b32 s57, s4, 1\n"
    "s_add_u32 s56, s56, s57\n"
    "s_and_b32 s56, s56, 15\n"
    "s_lshl1_add_u32 s56, s56, s13\n"
    "s_add_u32 s56, s56, 28\n"
    "s_and_b32 s56, s56, 31\n"
    "s_mul_i32 s56, s56, 57344\n"
    "s_lshl_b32 s57, s10, 8\n"
    "s_add_u32 s56, s56, s57\n"
    "s_lshl_b32 s57, s13, 12\n"
    "s_add_u32 m0, s57, 114688\n"
    ".Lh3_w_7:\n"
    "ds_read_b32 v20, v113 offset:8\n"
    "s_cmp_eq_u32 s3, 0\n"
    "s_cbranch_scc1 .Lh3_w_7x\n"
    "s_sub_u32 s3, s3, 1\n"
    "s_waitcnt lgkmcnt(0)\n"
    "v_cmp_gt_u32 vcc, 4, v20\n"
    "s_cbranch_vccnz .Lh3_w_7\n"
    ".Lh3_w_7x:\n"
    "ds_read_b128 v[16:19], v122 offset:192\n"
    "ds_read_b128 v[20:23], v122 offset:704\n"
    "ds_read_b128 v[24:27], v122 offset:1216\n"
    "ds_read_b128 v[28:31], v122 offset:1728\n"
    "ds_read_b128 v[32:35], v122 offset:2240\n"
    "ds_read_b128 v[36:39], v122 offset:2752\n"
    "ds_read_b128 v[40:43], v122 offset:3264\n"
    "ds_read_b128 v[44:47], v122 offset:3776\n"
    "s_waitcnt lgkmcnt(7)\n"
    "v_mfma_f32_16x16x32_bf16 v[80:83], v[128:131], v[16:19], 0\n"
    "v_mfma_f32_16x16x32_bf16 v[84:87], v[132:135], v[16:19], 0\n"
    "v_mfma_f32_16x16x32_bf16 v[88:91], v[136:139], v[16:19], 0\n"
    "v_mfma_f32_16x16x32_bf16 v[92:95], v[140:143], v[16:19], 0\n"
    "v_mfma_f32_16x16x32_bf16 v[96:99], v[144:147], v[16:19], 0\n"
    "v_mfma_f32_16x16x32_bf16 v[100:103], v[148:151], v[16:19], 0\n"
    "v_mfma_f32_16x16x32_bf16 v[104:107], v[152:155], v[16:19], 0\n"
    "v_mfma_f32_16x16x32_bf16 v[108:111], v[156:159], v[16:19], 0\n"
    "ds_read_b128 v[48:51], v122 offset:128\n"
    "s_waitcnt lgkmcnt(7)\n"
    "v_mfma_f32_16x16x32_bf16 v[84:87], v[128:131], v[20:23], v[84:87]\n"
    "v_mfma_f32_16x16x32_bf16 v[88:91], v[132:135], v[20:23], v[88:91]\n"
    "ds_read_b128 v[52:55], v122 offset:640\n"
    "v_mfma_f32_16x16x32_bf16 v[92:95], v[136:139], v[20:23], v[92:95]\n"
    "v_mfma_f32_16x16x32_bf16 v[96:99], v[140:143], v[20:23], v[96:99]\n"
    "ds_read_b128 v[56:59], v122 offset:1152\n"
    "v_mfma_f32_16x16x32_bf16 v[100:103], v[144:147], v[20:23], v[100:103]\n"
    "v_mfma_f32_16x16x32_bf16 v[104:107], v[148:151], v[20:23], v[104:107]\n"
    "ds_read_b128 v[60:63], v122 offset:1664\n"
    "v_mfma_f32_16x16x32_bf16 v[108:111], v[152:155], v[20:23], v[108:111]\n"
    "s_waitcnt lgkmcnt(9)\n"
    "v_mfma_f32_16x16x32_bf16 v[88:91], v[128:131], v[24:27], v[88:91]\n"
    "ds_read_b128 v[64:67], v122 offset:2176\n"
    "v_mfma_f32_16x16x32_bf16 v[92:95], v[132:135], v[24:27], v[92:95]\n"
    "v_mfma_f32_16x16x32_bf16 v[96:99], v[136:139], v[24:27], v[96:99]\n"
    "ds_read_b128 v[68:71], v122 offset:2688\n"
    "v_mfma_f32_16x16x32_bf16 v[100:103], v[140:143], v[24:27], v[100:103]\n"
    "v_mfma_f32_16x16x32_bf16 v[104:107], v[144:147], v[24:27], v[104:107]\n"
    "ds_read_b128 v[72:75], v122 offset:3200\n"
    "v_mfma_f32_16x16x32_bf16 v[108:111], v[148:151], v[24:27], v[108:111]\n"
    "s_waitcnt lgkmcnt(11)\n"
    "v_mfma_f32_16x16x32_bf16 v[92:95], v[128:131], v[28:31], v[92:95]\n"
    "ds_read_b128 v[76:79], v122 offset:3712\n"
    "v_mfma_f32_16x16x32_bf16 v[96:99], v[132:135], v[28:31], v[96:99]\n"
    "v_mfma_f32_16x16x32_bf16 v[100:103], v[136:139], v[28:31], v[100:103]\n"
    "v_mfma_f32_16x16x32_bf16 v[104:107], v[140:143], v[28:31], v[104:107]\n"
    "v_mfma_f32_16x16x32_bf16 v[108:111], v[144:147], v[28:31], v[108:111]\n"
    "s_waitcnt lgkmcnt(11)\n"
    "v_mfma_f32_16x16x32_bf16 v[96:99], v[128:131], v[32:35], v[96:99]\n"
    "v_mfma_f32_16x16x32_bf16 v[100:103], v[132:135], v[32:35], v[100:103]\n"
    "v_mfma_f32_16x16x32_bf16 v[104:107], v[136:139], v[32:35], v[104:107]\n"
    "v_mfma_f32_16x16x32_bf16 v[108:111], v[140:143], v[32:35], v[108:111]\n"
    "s_waitcnt lgkmcnt(10)\n"
    "v_mfma_f32_16x16x32_bf16 v[100:103], v[128:131], v[36:39], v[100:103]\n"
    "v_mfma_f32_16x16x32_bf16 v[104:107], v[132:135], v[36:39], v[104:107]\n"
    "v_mfma_f32_16x16x32_bf16 v[108:111], v[136:139], v[36:39], v[108:111]\n"
    "s_waitcnt lgkmcnt(9)\n"
    "v_mfma_f32_16x16x32_bf16 v[104:107], v[128:131], v[40:43], v[104:107]\n"
    "v_mfma_f32_16x16x32_bf16 v[108:111], v[132:135], v[40:43], v[108:111]\n"
    "s_waitcnt lgkmcnt(8)\n"
    "v_mfma_f32_16x16x32_bf16 v[108:111], v[128:131], v[44:47], v[108:111]\n"
    "s_waitcnt lgkmcnt(4)\n"
    "v_mfma_f32_16x16x32_bf16 v[80:83], v[160:163], v[48:51], v[80:83]\n"
    "v_mfma_f32_16x16x32_bf16 v[84:87], v[164:167], v[48:51], v[84:87]\n"
    "v_mfma_f32_16x16x32_bf16 v[88:91], v[168:171], v[48:51], v[88:91]\n"
    "v_mfma_f32_16x16x32_bf16 v[92:95], v[172:175], v[48:51], v[92:95]\n"
    "v_mfma_f32_16x16x32_bf16 v[96:99], v[176:179], v[48:51], v[96:99]\n"
    "v_mfma_f32_16x16x32_bf16 v[100:103], v[180:183], v[48:51], v[100:103]\n"
    "v_mfma_f32_16x16x32_bf16 v[104:107], v[184:187], v[48:51], v[104:107]\n"
    "v_mfma_f32_16x16x32_bf16 v[108:111], v[188:191], v[48:51], v[108:111]\n"
    "ds_read_b128 v[16:19], v122 offset:64\n"
    "v_mfma_f32_16x16x32_bf16 v[84:87], v[160:163], v[52:55], v[84:87]\n"
    "v_mfma_f32_16x16x32_bf16 v[88:91], v[164:167], v[52:55], v[88:91]\n"
    "ds_read_b128 v[20:23], v122 offset:576\n"
    "v_mfma_f32_16x16x32_bf16 v[92:95], v[168:171], v[52:55], v[92:95]\n"
    "v_mfma_f32_16x16x32_bf16 v[96:99], v[172:175], v[52:55], v[96:99]\n"
    "ds_read_b128 v[24:27], v122 offset:1088\n"
    "v_mfma_f32_16x16x32_bf16 v[100:103], v[176:179], v[52:55], v[100:103]\n"
    "v_mfma_f32_16x16x32_bf16 v[104:107], v[180:183], v[52:55], v[104:107]\n"
    "ds_read_b128 v[28:31], v122 offset:1600\n"
    "v_mfma_f32_16x16x32_bf16 v[108:111], v[184:187], v[52:55], v[108:111]\n"
    "v_mfma_f32_16x16x32_bf16 v[88:91], v[160:163], v[56:59], v[88:91]\n"
    "ds_read_b128 v[32:35], v122 offset:2112\n"
    "v_mfma_f32_16x16x32_bf16 v[92:95], v[164:167], v[56:59], v[92:95]\n"
    "v_mfma_f32_16x16x32_bf16 v[96:99], v[168:171], v[56:59], v[96:99]\n"
    "ds_read_b128 v[36:39], v122 offset:2624\n"
    "v_mfma_f32_16x16x32_bf16 v[100:103], v[172:175], v[56:59], v[100:103]\n"
    "v_mfma_f32_16x16x32_bf16 v[104:107], v[176:179], v[56:59], v[104:107]\n"
    "ds_read_b128 v[40:43], v122 offset:3136\n"
    "v_mfma_f32_16x16x32_bf16 v[108:111], v[180:183], v[56:59], v[108:111]\n"
    "v_mfma_f32_16x16x32_bf16 v[92:95], v[160:163], v[60:63], v[92:95]\n"
    "ds_read_b128 v[44:47], v122 offset:3648\n"
    "v_mfma_f32_16x16x32_bf16 v[96:99], v[164:167], v[60:63], v[96:99]\n"
    "v_mfma_f32_16x16x32_bf16 v[100:103], v[168:171], v[60:63], v[100:103]\n"
    "v_mfma_f32_16x16x32_bf16 v[104:107], v[172:175], v[60:63], v[104:107]\n"
    "v_mfma_f32_16x16x32_bf16 v[108:111], v[176:179], v[60:63], v[108:111]\n"
    "s_waitcnt lgkmcnt(8)\n"
    "v_mfma_f32_16x16x32_bf16 v[96:99], v[160:163], v[64:67], v[96:99]\n"
    "v_mfma_f32_16x16x32_bf16 v[100:103], v[164:167], v[64:67], v[100:103]\n"
    "v_mfma_f32_16x16x32_bf16 v[104:107], v[168:171], v[64:67], v[104:107]\n"
    "v_mfma_f32_16x16x32_bf16 v[108:111], v[172:175], v[64:67], v[108:111]\n"
    "v_mfma_f32_16x16x32_bf16 v[100:103], v[160:163], v[68:71], v[100:103]\n"
    "v_mfma_f32_16x16x32_bf16 v[104:107], v[164:167], v[68:71], v[104:107]\n"
    "v_mfma_f32_16x16x32_bf16 v[108:111], v[168:171], v[68:71], v[108:111]\n"
    "v_mfma_f32_16x16x32_bf16 v[104:107], v[160:163], v[72:75], v[104:107]\n"
    "v_mfma_f32_16x16x32_bf16 v[108:111], v[164:167], v[72:75], v[108:111]\n"
    "v_mfma_f32_16x16x32_bf16 v[108:111], v[160:163], v[76:79], v[108:111]\n"
    "s_waitcnt lgkmcnt(4)\n"
    "v_mfma_f32_16x16x32_bf16 v[80:83], v[192:195], v[16:19], v[80:83]\n"
    "v_mfma_f32_16x16x32_bf16 v[84:87], v[196:199], v[16:19], v[84:87]\n"
    "v_mfma_f32_16x16x32_bf16 v[88:91], v[200:203], v[16:19], v[88:91]\n"
    "v_mfma_f32_16x16x32_bf16 v[92:95], v[204:207], v[16:19], v[92:95]\n"
    "v_mfma_f32_16x16x32_bf16 v[96:99], v[208:211], v[16:19], v[96:99]\n"
    "v_mfma_f32_16x16x32_bf16 v[100:103], v[212:215], v[16:19], v[100:103]\n"
    "v_mfma_f32_16x16x32_bf16 v[104:107], v[216:219], v[16:19], v[104:107]\n"
    "v_mfma_f32_16x16x32_bf16 v[108:111], v[220:223], v[16:19], v[108:111]\n"
    "ds_read_b128 v[48:51], v122 offset:0\n"
    "v_mfma_f32_16x16x32_bf16 v[84:87], v[192:195], v[20:23], v[84:87]\n"
    "v_mfma_f32_16x16x32_bf16 v[88:91], v[196:199], v[20:23], v[88:91]\n"
    "ds_read_b128 v[52:55], v122 offset:512\n"
    "v_mfma_f32_16x16x32_bf16 v[92:95], v[200:203], v[20:23], v[92:95]\n"
    "v_mfma_f32_16x16x32_bf16 v[96:99], v[204:207], v[20:23], v[96:99]\n"
    "ds_read_b128 v[56:59], v122 offset:1024\n"
    "v_mfma_f32_16x16x32_bf16 v[100:103], v[208:211], v[20:23], v[100:103]\n"
    "v_mfma_f32_16x16x32_bf16 v[104:107], v[212:215], v[20:23], v[104:107]\n"
    "ds_read_b128 v[60:63], v122 offset:1536\n"
    "v_mfma_f32_16x16x32_bf16 v[108:111], v[216:219], v[20:23], v[108:111]\n"
    "v_mfma_f32_16x16x32_bf16 v[88:91], v[192:195], v[24:27], v[88:91]\n"
    "ds_read_b128 v[64:67], v122 offset:2048\n"
    "v_mfma_f32_16x16x32_bf16 v[92:95], v[196:199], v[24:27], v[92:95]\n"
    "v_mfma_f32_16x16x32_bf16 v[96:99], v[200:203], v[24:27], v[96:99]\n"
    "ds_read_b128 v[68:71], v122 offset:2560\n"
    "v_mfma_f32_16x16x32_bf16 v[100:103], v[204:207], v[24:27], v[100:103]\n"
    "v_mfma_f32_16x16x32_bf16 v[104:107], v[208:211], v[24:27], v[104:107]\n"
    "ds_read_b128 v[72:75], v122 offset:3072\n"
    "v_mfma_f32_16x16x32_bf16 v[108:111], v[212:215], v[24:27], v[108:111]\n"
    "v_mfma_f32_16x16x32_bf16 v[92:95], v[192:195], v[28:31], v[92:95]\n"
    "ds_read_b128 v[76:79], v122 offset:3584\n"
    "v_mfma_f32_16x16x32_bf16 v[96:99], v[196:199], v[28:31], v[96:99]\n"
    "v_mfma_f32_16x16x32_bf16 v[100:103], v[200:203], v[28:31], v[100:103]\n"
    "v_mfma_f32_16x16x32_bf16 v[104:107], v[204:207], v[28:31], v[104:107]\n"
    "v_mfma_f32_16x16x32_bf16 v[108:111], v[208:211], v[28:31], v[108:111]\n"
    "s_waitcnt lgkmcnt(8)\n"
    "v_mfma_f32_16x16x32_bf16 v[96:99], v[192:195], v[32:35], v[96:99]\n"
    "v_mfma_f32_16x16x32_bf16 v[100:103], v[196:199], v[32:35], v[100:103]\n"
    "v_mfma_f32_16x16x32_bf16 v[104:107], v[200:203], v[32:35], v[104:107]\n"
    "v_mfma_f32_16x16x32_bf16 v[108:111], v[204:207], v[32:35], v[108:111]\n"
    "v_mfma_f32_16x16x32_bf16 v[100:103], v[192:195], v[36:39], v[100:103]\n"
    "v_mfma_f32_16x16x32_bf16 v[104:107], v[196:199], v[36:39], v[104:107]\n"
    "v_mfma_f32_16x16x32_bf16 v[108:111], v[200:203], v[36:39], v[108:111]\n"
    "v_mfma_f32_16x16x32_bf16 v[104:107], v[192:195], v[40:43], v[104:107]\n"
    "v_mfma_f32_16x16x32_bf16 v[108:111], v[196:199], v[40:43], v[108:111]\n"
    "v_mfma_f32_16x16x32_bf16 v[108:111], v[192:195], v[44:47], v[108:111]\n"
    "s_waitcnt lgkmcnt(0)\n"
    "v_mfma_f32_16x16x32_bf16 v[80:83], v[224:227], v[48:51], v[80:83]\n"
    "v_mfma_f32_16x16x32_bf16 v[84:87], v[228:231], v[48:51], v[84:87]\n"
    "v_mfma_f32_16x16x32_bf16 v[84:87], v[224:227], v[52:55], v[84:87]\n"
    "v_mfma_f32_16x16x32_bf16 v[88:91], v[232:235], v[48:51], v[88:91]\n"
    "v_mfma_f32_16x16x32_bf16 v[88:91], v[228:231], v[52:55], v[88:91]\n"
    "v_mfma_f32_16x16x32_bf16 v[88:91], v[224:227], v[56:59], v[88:91]\n"
    "v_mfma_f32_16x16x32_bf16 v[92:95], v[236:239], v[48:51], v[92:95]\n"
    "v_mfma_f32_16x16x32_bf16 v[92:95], v[232:235], v[52:55], v[92:95]\n"
    "s_nop 0\n"
    "ds_write_b128 v123, v[80:83] offset:0\n"
    "v_mfma_f32_16x16x32_bf16 v[92:95], v[228:231], v[56:59], v[92:95]\n"
    "v_mfma_f32_16x16x32_bf16 v[92:95], v[224:227], v[60:63], v[92:95]\n"
    "ds_write_b128 v123, v[84:87] offset:1024\n"
    "v_mfma_f32_16x16x32_bf16 v[96:99], v[240:243], v[48:51], v[96:99]\n"
    "v_mfma_f32_16x16x32_bf16 v[96:99], v[236:239], v[52:55], v[96:99]\n"
    "v_mfma_f32_16x16x32_bf16 v[96:99], v[232:235], v[56:59], v[96:99]\n"
    "ds_write_b128 v123, v[88:91] offset:2048\n"
    "v_mfma_f32_16x16x32_bf16 v[96:99], v[228:231], v[60:63], v[96:99]\n"
    "v_mfma_f32_16x16x32_bf16 v[96:99], v[224:227], v[64:67], v[96:99]\n"
    "v_mfma_f32_16x16x32_bf16 v[100:103], v[244:247], v[48:51], v[100:103]\n"
    "v_mfma_f32_16x16x32_bf16 v[100:103], v[240:243], v[52:55], v[100:103]\n"
    "ds_write_b128 v123, v[92:95] offset:3072\n"
    "v_mfma_f32_16x16x32_bf16 v[100:103], v[236:239], v[56:59], v[100:103]\n"
    "v_mfma_f32_16x16x32_bf16 v[100:103], v[232:235], v[60:63], v[100:103]\n"
    "v_mfma_f32_16x16x32_bf16 v[100:103], v[228:231], v[64:67], v[100:103]\n"
    "v_mfma_f32_16x16x32_bf16 v[100:103], v[224:227], v[68:71], v[100:103]\n"
    "v_mfma_f32_16x16x32_bf16 v[104:107], v[248:251], v[48:51], v[104:107]\n"
    "ds_write_b128 v123, v[96:99] offset:4096\n"
    "v_mfma_f32_16x16x32_bf16 v[104:107], v[244:247], v[52:55], v[104:107]\n"
    "v_mfma_f32_16x16x32_bf16 v[104:107], v[240:243], v[56:59], v[104:107]\n"
    "v_mfma_f32_16x16x32_bf16 v[104:107], v[236:239], v[60:63], v[104:107]\n"
    "v_mfma_f32_16x16x32_bf16 v[104:107], v[232:235], v[64:67], v[104:107]\n"
    "v_mfma_f32_16x16x32_bf16 v[104:107], v[228:231], v[68:71], v[104:107]\n"
    "v_mfma_f32_16x16x32_bf16 v[104:107], v[224:227], v[72:75], v[104:107]\n"
    "ds_write_b128 v123, v[100:103] offset:5120\n"
    "v_mfma_f32_16x16x32_bf16 v[108:111], v[252:255], v[48:51], v[108:111]\n"
    "v_mfma_f32_16x16x32_bf16 v[108:111], v[248:251], v[52:55], v[108:111]\n"
    "v_mfma_f32_16x16x32_bf16 v[108:111], v[244:247], v[56:59], v[108:111]\n"
    "v_mfma_f32_16x16x32_bf16 v[108:111], v[240:243], v[60:63], v[108:111]\n"
    "v_mfma_f32_16x16x32_bf16 v[108:111], v[236:239], v[64:67], v[108:111]\n"
    "v_mfma_f32_16x16x32_bf16 v[108:111], v[232:235], v[68:71], v[108:111]\n"
    "v_mfma_f32_16x16x32_bf16 v[108:111], v[228:231], v[72:75], v[108:111]\n"
    "ds_write_b128 v123, v[104:107] offset:6144\n"
    "v_mfma_f32_16x16x32_bf16 v[108:111], v[224:227], v[76:79], v[108:111]\n"
    "s_nop 7\n"
    "ds_write_b128 v123, v[108:111] offset:7168\n"
    "s_waitcnt lgkmcnt(0)\n"
    "s_barrier\n"
    "s_cmp_ge_u32 s5, 28\n"
    "s_cbranch_scc1 .Lh3_A_spare\n"
    "s_waitcnt vmcnt(7)\n"
    "buffer_load_dwordx4 v2, s[48:51], s56 offen lds\n"
    "s_addk_i32 m0, 0x400\n"
    "s_addk_i32 s56, 14336\n"
    "s_waitcnt vmcnt(7)\n"
    "buffer_load_dwordx4 v3, s[48:51], s56 offen lds\n"
    "s_addk_i32 m0, 0x400\n"
    "s_addk_i32 s56, 14336\n"
    "s_waitcnt vmcnt(7)\n"
    "buffer_load_dwordx4 v2, s[48:51], s56 offen lds\n"
    "s_addk_i32 m0, 0x400\n"
    "s_addk_i32 s56, 14336\n"
    "s_waitcnt vmcnt(7)\n"
    "buffer_load_dwordx4 v3, s[48:51], s56 offen lds\n"
    "s_addk_i32 m0, 0x400\n"
    "s_addk_i32 s56, 14336\n"
    "v_lshrrev_b32 v16, 2, v1\n"
    "v_and_b32 v17, 7, v16\n"
    "s_lshl_b32 s57, s13, 1\n"
    "v_xor_b32 v17, s57, v17\n"
    "v_lshlrev_b32 v16, 8, v16\n"
    "v_lshl_or_b32 v16, v17, 5, v16\n"
    "v_and_b32 v17, 3, v1\n"
    "v_lshl_or_b32 v4, v17, 3, v16\n"
    "v_xor_b32 v5, 32, v4\n"
    "v_add_u32 v6, 0x10000, v4\n"
    "v_add_u32 v7, 0x10000, v5\n"
    "s_lshl_b32 s58, s9, 9\n"
    "s_add_u32 s58, s58, 32768\n"
    "s_mul_i32 s59, s9, 14\n"
    "s_add_u32 s59, s59, s10\n"
    "s_lshl2_add_u32 s59, s59, s13\n"
    "s_lshl_b32 s59, s59, 7\n"
    "s_add_u32 s59, s59, 13860864\n"
    "v_lshrrev_b32 v16, 2, v1\n"
    "v_lshlrev_b32 v16, 7, v16\n"
    "v_and_b32 v17, 3, v1\n"
    "v_lshl_or_b32 v16, v17, 3, v16\n"
    "v_bfe_u32 v17, v1, 3, 2\n"
    "v_lshl_or_b32 v8, v17, 5, v16\n"
    "v_xor_b32 v9, 32, v8\n"
    "v_xor_b32 v10, 64, v8\n"
    "v_xor_b32 v11, 96, v8\n"
    "v_add_u32 v12, 0x10000, v8\n"
    "v_add_u32 v13, 0x10000, v9\n"
    "v_add_u32 v14, 0x10000, v10\n"
    "v_add_u32 v15, 0x10000, v11\n"
    "s_mul_i32 s56, s9, 56\n"
    "s_add_u32 s56, s56, s10\n"
    "s_lshl2_add_u32 s56, s56, s13\n"
    "s_lshl_b32 s56, s56, 11\n"
    "s_add_u32 s56, s56, 6389760\n"
    "v_lshlrev_b32 v2, 4, v1\n"
    "v_and_b32 v3, 15, v1\n"
    "v_lshlrev_b32 v3, 4, v3\n"
    "v_mov_b32 v16, 0x3f803f80\n"
    "v_mov_b32 v17, 0x3f803f80\n"
    "v_mov_b32 v18, 0x3f803f80\n"
    "v_mov_b32 v19, 0x3f803f80\n"
    "s_waitcnt vmcnt(0)\n"
    "s_barrier\n"
    "ds_read_b64_tr_b16 v[128:129], v4 offset:0\n"
    "ds_read_b64_tr_b16 v[130:131], v4 offset:4096\n"
    "ds_read_b64_tr_b16 v[132:133], v5 offset:0\n"
    "ds_read_b64_tr_b16 v[134:135], v5 offset:4096\n"
    "ds_read_b64_tr_b16 v[136:137], v4 offset:8192\n"
    "ds_read_b64_tr_b16 v[138:139], v4 offset:12288\n"
    "ds_read_b64_tr_b16 v[140:141], v5 offset:8192\n"
    "ds_read_b64_tr_b16 v[142:143], v5 offset:12288\n"
    "ds_read_b64_tr_b16 v[144:145], v4 offset:16384\n"
    "ds_read_b64_tr_b16 v[146:147], v4 offset:20480\n"
    "ds_read_b64_tr_b16 v[148:149], v5 offset:16384\n"
    "ds_read_b64_tr_b16 v[150:151], v5 offset:20480\n"
    "ds_read_b64_tr_b16 v[152:153], v4 offset:24576\n"
    "ds_read_b64_tr_b16 v[154:155], v4 offset:28672\n"
    "ds_read_b64_tr_b16 v[156:157], v5 offset:24576\n"
    "ds_read_b64_tr_b16 v[158:159], v5 offset:28672\n"
    "ds_read_b64_tr_b16 v[160:161], v4 offset:32768\n"
    "ds_read_b64_tr_b16 v[162:163], v4 offset:36864\n"
    "ds_read_b64_tr_b16 v[164:165], v5 offset:32768\n"
    "ds_read_b64_tr_b16 v[166:167], v5 offset:36864\n"
    "ds_read_b64_tr_b16 v[168:169], v4 offset:40960\n"
    "ds_read_b64_tr_b16 v[170:171], v4 offset:45056\n"
    "ds_read_b64_tr_b16 v[172:173], v5 offset:40960\n"
    "ds_read_b64_tr_b16 v[174:175], v5 offset:45056\n"
    "ds_read_b64_tr_b16 v[176:177], v4 offset:49152\n"
    "ds_read_b64_tr_b16 v[178:179], v4 offset:53248\n"
    "ds_read_b64_tr_b16 v[180:181], v5 offset:49152\n"
    "ds_read_b64_tr_b16 v[182:183], v5 offset:53248\n"
    "ds_read_b64_tr_b16 v[184:185], v4 offset:57344\n"
    "ds_read_b64_tr_b16 v[186:187], v4 offset:61440\n"
    "ds_read_b64_tr_b16 v[188:189], v5 offset:57344\n"
    "ds_read_b64_tr_b16 v[190:191], v5 offset:61440\n"
    "ds_read_b64_tr_b16 v[192:193], v6 offset:0\n"
    "ds_read_b64_tr_b16 v[194:195], v6 offset:4096\n"
    "ds_read_b64_tr_b16 v[196:197], v7 offset:0\n"
    "ds_read_b64_tr_b16 v[198:199], v7 offset:4096\n"
    "ds_read_b64_tr_b16 v[200:201], v6 offset:8192\n"
    "ds_read_b64_tr_b16 v[202:203], v6 offset:12288\n"
    "ds_read_b64_tr_b16 v[204:205], v7 offset:8192\n"
    "ds_read_b64_tr_b16 v[206:207], v7 offset:12288\n"
    "ds_read_b64_tr_b16 v[208:209], v6 offset:16384\n"
    "ds_read_b64_tr_b16 v[210:211], v6 offset:20480\n"
    "ds_read_b64_tr_b16 v[212:213], v7 offset:16384\n"
    "ds_read_b64_tr_b16 v[214:215], v7 offset:20480\n"
    "ds_read_b64_tr_b16 v[216:217], v6 offset:24576\n"
    "ds_read_b64_tr_b16 v[218:219], v6 offset:28672\n"
    "ds_read_b64_tr_b16 v[220:221], v7 offset:24576\n"
    "ds_read_b64_tr_b16 v[222:223], v7 offset:28672\n"
    "ds_read_b64_tr_b16 v[224:225], v6 offset:32768\n"
    "ds_read_b64_tr_b16 v[226:227], v6 offset:36864\n"
    "ds_read_b64_tr_b16 v[228:229], v7 offset:32768\n"
    "ds_read_b64_tr_b16 v[230:231], v7 offset:36864\n"
    "ds_read_b64_tr_b16 v[232:233], v6 offset:40960\n"
    "ds_read_b64_tr_b16 v[234:235], v6 offset:45056\n"
    "ds_read_b64_tr_b16 v[236:237], v7 offset:40960\n"
    "ds_read_b64_tr_b16 v[238:239], v7 offset:45056\n"
    "ds_read_b64_tr_b16 v[240:241], v6 offset:49152\n"
    "ds_read_b64_tr_b16 v[242:243], v6 offset:53248\n"
    "ds_read_b64_tr_b16 v[244:245], v7 offset:49152\n"
    "ds_read_b64_tr_b16 v[246:247], v7 offset:53248\n"
    "ds_read_b64_tr_b16 v[248:249], v6 offset:57344\n"
    "ds_read_b64_tr_b16 v[250:251], v6 offset:61440\n"
    "ds_read_b64_tr_b16 v[252:253], v7 offset:57344\n"
    "ds_read_b64_tr_b16 v[254:255], v7 offset:61440\n"
    "s_waitcnt lgkmcnt(0)\n"
    "s_barrier\n"
    "v_mfma_f32_16x16x32_bf16 v[20:23], v[128:131], v[16:19], 0\n"
    "v_mfma_f32_16x16x32_bf16 v[24:27], v[132:135], v[16:19], 0\n"
    "v_mfma_f32_16x16x32_bf16 v[20:23], v[136:139], v[16:19], v[20:23]\n"
    "v_mfma_f32_16x16x32_bf16 v[24:27], v[140:143], v[16:19], v[24:27]\n"
    "v_mfma_f32_16x16x32_bf16 v[20:23], v[144:147], v[16:19], v[20:23]\n"
    "v_mfma_f32_16x16x32_bf16 v[24:27], v[148:151], v[16:19], v[24:27]\n"
    "v_mfma_f32_16x16x32_bf16 v[20:23], v[152:155], v[16:19], v[20:23]\n"
    "v_mfma_f32_16x16x32_bf16 v[24:27], v[156:159], v[16:19], v[24:27]\n"
    "v_mfma_f32_16x16x32_bf16 v[20:23], v[160:163], v[16:19], v[20:23]\n"
    "v_mfma_f32_16x16x32_bf16 v[24:27], v[164:167], v[16:19], v[24:27]\n"
    "v_mfma_f32_16x16x32_bf16 v[20:23], v[168:171], v[16:19], v[20:23]\n"
    "v_mfma_f32_16x16x32_bf16 v[24:27], v[172:175], v[16:19], v[24:27]\n"
    "v_mfma_f32_16x16x32_bf16 v[20:23], v[176:179], v[16:19], v[20:23]\n"
    "v_mfma_f32_16x16x32_bf16 v[24:27], v[180:183], v[16:19], v[24:27]\n"
    "v_mfma_f32_16x16x32_bf16 v[20:23], v[184:187], v[16:19], v[20:23]\n"
    "v_mfma_f32_16x16x32_bf16 v[24:27], v[188:191], v[16:19], v[24:27]\n"
    "v_mfma_f32_16x16x32_bf16 v[20:23], v[192:195], v[16:19], v[20:23]\n"
    "v_mfma_f32_16x16x32_bf16 v[24:27], v[196:199], v[16:19], v[24:27]\n"
    "v_mfma_f32_16x16x32_bf16 v[20:23], v[200:203], v[16:19], v[20:23]\n"
    "v_mfma_f32_16x16x32_bf16 v[24:27], v[204:207], v[16:19], v[24:27]\n"
    "v_mfma_f32_16x16x32_bf16 v[20:23], v[208:211], v[16:19], v[20:23]\n"
    "v_mfma_f32_16x16x32_bf16 v[24:27], v[212:215], v[16:19], v[24:27]\n"
    "v_mfma_f32_16x16x32_bf16 v[20:23], v[216:219], v[16:19], v[20:23]\n"
    "v_mfma_f32_16x16x32_bf16 v[24:27], v[220:223], v[16:19], v[24:27]\n"
    "v_mfma_f32_16x16x32_bf16 v[20:23], v[224:227], v[16:19], v[20:23]\n"
    "v_mfma_f32_16x16x32_bf16 v[24:27], v[228:231], v[16:19], v[24:27]\n"
    "v_mfma_f32_16x16x32_bf16 v[20:23], v[232:235], v[16:19], v[20:23]\n"
    "v_mfma_f32_16x16x32_bf16 v[24:27], v[236:239], v[16:19], v[24:27]\n"
    "v_mfma_f32_16x16x32_bf16 v[20:23], v[240:243], v[16:19], v[20:23]\n"
    "v_mfma_f32_16x16x32_bf16 v[24:27], v[244:247], v[16:19], v[24:27]\n"
    "v_mfma_f32_16x16x32_bf16 v[20:23], v[248:251], v[16:19], v[20:23]\n"
    "v_mfma_f32_16x16x32_bf16 v[24:27], v[252:255], v[16:19], v[24:27]\n"
    "s_barrier\n"
    "ds_read_b64_tr_b16 v[28:29], v8 offset:0\n"
    "ds_read_b64_tr_b16 v[30:31], v8 offset:2048\n"
    "ds_read_b64_tr_b16 v[32:33], v9 offset:0\n"
    "ds_read_b64_tr_b16 v[34:35], v9 offset:2048\n"
    "ds_read_b64_tr_b16 v[36:37], v10 offset:0\n"
    "ds_read_b64_tr_b16 v[38:39], v10 offset:2048\n"
    "ds_read_b64_tr_b16 v[40:41], v11 offset:0\n"
    "ds_read_b64_tr_b16 v[42:43], v11 offset:2048\n"
    "ds_read_b64_tr_b16 v[44:45], v8 offset:4096\n"
    "ds_read_b64_tr_b16 v[46:47], v8 offset:6144\n"
    "ds_read_b64_tr_b16 v[48:49], v9 offset:4096\n"
    "ds_read_b64_tr_b16 v[50:51], v9 offset:6144\n"
    "ds_read_b64_tr_b16 v[52:53], v10 offset:4096\n"
    "ds_read_b64_tr_b16 v[54:55], v10 offset:6144\n"
    "ds_read_b64_tr_b16 v[56:57], v11 offset:4096\n"
    "ds_read_b64_tr_b16 v[58:59], v11 offset:6144\n"
    "s_waitcnt lgkmcnt(8)\n"
    "v_mfma_f32_16x16x32_bf16 v[64:67], v[128:131], v[28:31], 0\n"
    "ds_read_b64_tr_b16 v[112:113], v8 offset:8192\n"
    "v_mfma_f32_16x16x32_bf16 v[68:71], v[132:135], v[28:31], 0\n"
    "ds_read_b64_tr_b16 v[114:115], v8 offset:10240\n"
    "v_mfma_f32_16x16x32_bf16 v[72:75], v[128:131], v[32:35], 0\n"
    "ds_read_b64_tr_b16 v[116:117], v9 offset:8192\n"
    "v_mfma_f32_16x16x32_bf16 v[76:79], v[132:135], v[32:35], 0\n"
    "ds_read_b64_tr_b16 v[118:119], v9 offset:10240\n"
    "v_mfma_f32_16x16x32_bf16 v[80:83], v[128:131], v[36:39], 0\n"
    "ds_read_b64_tr_b16 v[120:121], v10 offset:8192\n"
    "v_mfma_f32_16x16x32_bf16 v[84:87], v[132:135], v[36:39], 0\n"
    "ds_read_b64_tr_b16 v[122:123], v10 offset:10240\n"
    "v_mfma_f32_16x16x32_bf16 v[88:91], v[128:131], v[40:43], 0\n"
    "ds_read_b64_tr_b16 v[124:125], v11 offset:8192\n"
    "v_mfma_f32_16x16x32_bf16 v[92:95], v[132:135], v[40:43], 0\n"
    "ds_read_b64_tr_b16 v[126:127], v11 offset:10240\n"
    "s_waitcnt lgkmcnt(8)\n"
    "v_mfma_f32_16x16x32_bf16 v[64:67], v[136:139], v[44:47], v[64:67]\n"
    "ds_read_b64_tr_b16 v[28:29], v8 offset:12288\n"
    "v_mfma_f32_16x16x32_bf16 v[68:71], v[140:143], v[44:47], v[68:71]\n"
    "ds_read_b64_tr_b16 v[30:31], v8 offset:14336\n"
    "v_mfma_f32_16x16x32_bf16 v[72:75], v[136:139], v[48:51], v[72:75]\n"
    "ds_read_b64_tr_b16 v[32:33], v9 offset:12288\n"
    "v_mfma_f32_16x16x32_bf16 v[76:79], v[140:143], v[48:51], v[76:79]\n"
    "ds_read_b64_tr_b16 v[34:35], v9 offset:14336\n"
    "v_mfma_f32_16x16x32_bf16 v[80:83], v[136:139], v[52:55], v[80:83]\n"
    "ds_read_b64_tr_b16 v[36:37], v10 offset:12288\n"
    "v_mfma_f32_16x16x32_bf16 v[84:87], v[140:143], v[52:55], v[84:87]\n"
    "ds_read_b64_tr_b16 v[38:39], v10 offset:14336\n"
    "v_mfma_f32_16x16x32_bf16 v[88:91], v[136:139], v[56:59], v[88:91]\n"
    "ds_read_b64_tr_b16 v[40:41], v11 offset:12288\n"
    "v_mfma_f32_16x16x32_bf16 v[92:95], v[140:143], v[56:59], v[92:95]\n"
    "ds_read_b64_tr_b16 v[42:43], v11 offset:14336\n"
    "s_waitcnt lgkmcnt(8)\n"
    "s_barrier\n"
    "v_mfma_f32_16x16x32_bf16 v[64:67], v[144:147], v[112:115], v[64:67]\n"
    "ds_read_b64_tr_b16 v[44:45], v8 offset:16384\n"
    "v_mfma_f32_16x16x32_bf16 v[68:71], v[148:151], v[112:115], v[68:71]\n"
    "ds_read_b64_tr_b16 v[46:47], v8 offset:18432\n"
    "v_mfma_f32_16x16x32_bf16 v[72:75], v[144:147], v[116:119], v[72:75]\n"
    "ds_read_b64_tr_b16 v[48:49], v9 offset:16384\n"
    "v_mfma_f32_16x16x32_bf16 v[76:79], v[148:151], v[116:119], v[76:79]\n"
    "ds_read_b64_tr_b16 v[50:51], v9 offset:18432\n"
    "v_mfma_f32_16x16x32_bf16 v[80:83], v[144:147], v[120:123], v[80:83]\n"
    "ds_read_b64_tr_b16 v[52:53], v10 offset:16384\n"
    "v_mfma_f32_16x16x32_bf16 v[84:87], v[148:151], v[120:123], v[84:87]\n"
    "ds_read_b64_tr_b16 v[54:55], v10 offset:18432\n"
    "v_mfma_f32_16x16x32_bf16 v[88:91], v[144:147], v[124:127], v[88:91]\n"
    "ds_read_b64_tr_b16 v[56:57], v11 offset:16384\n"
    "v_mfma_f32_16x16x32_bf16 v[92:95], v[148:151], v[124:127], v[92:95]\n"
    "ds_read_b64_tr_b16 v[58:59], v11 offset:18432\n"
    "s_waitcnt lgkmcnt(8)\n"
    "v_mfma_f32_16x16x32_bf16 v[64:67], v[152:155], v[28:31], v[64:67]\n"
    "ds_read_b64_tr_b16 v[112:113], v8 offset:20480\n"
    "v_mfma_f32_16x16x32_bf16 v[68:71], v[156:159], v[28:31], v[68:71]\n"
    "ds_read_b64_tr_b16 v[114:115], v8 offset:22528\n"
    "v_mfma_f32_16x16x32_bf16 v[72:75], v[152:155], v[32:35], v[72:75]\n"
    "ds_read_b64_tr_b16 v[116:117], v9 offset:20480\n"
    "v_mfma_f32_16x16x32_bf16 v[76:79], v[156:159], v[32:35], v[76:79]\n"
    "ds_read_b64_tr_b16 v[118:119], v9 offset:22528\n"
    "v_mfma_f32_16x16x32_bf16 v[80:83], v[152:155], v[36:39], v[80:83]\n"
    "ds_read_b64_tr_b16 v[120:121], v10 offset:20480\n"
    "v_mfma_f32_16x16x32_bf16 v[84:87], v[156:159], v[36:39], v[84:87]\n"
    "ds_read_b64_tr_b16 v[122:123], v10 offset:22528\n"
    "v_mfma_f32_16x16x32_bf16 v[88:91], v[152:155], v[40:43], v[88:91]\n"
    "ds_read_b64_tr_b16 v[124:125], v11 offset:20480\n"
    "v_mfma_f32_16x16x32_bf16 v[92:95], v[156:159], v[40:43], v[92:95]\n"
    "ds_read_b64_tr_b16 v[126:127], v11 offset:22528\n"
    "s_waitcnt lgkmcnt(8)\n"
    "v_mfma_f32_16x16x32_bf16 v[64:67], v[160:163], v[44:47], v[64:67]\n"
    "ds_read_b64_tr_b16 v[28:29], v8 offset:24576\n"
    "v_mfma_f32_16x16x32_bf16 v[68:71], v[164:167], v[44:47], v[68:71]\n"
    "ds_read_b64_tr_b16 v[30:31], v8 offset:26624\n"
    "v_mfma_f32_16x16x32_bf16 v[72:75], v[160:163], v[48:51], v[72:75]\n"
    "ds_read_b64_tr_b16 v[32:33], v9 offset:24576\n"
    "v_mfma_f32_16x16x32_bf16 v[76:79], v[164:167], v[48:51], v[76:79]\n"
    "ds_read_b64_tr_b16 v[34:35], v9 offset:26624\n"
    "v_mfma_f32_16x16x32_bf16 v[80:83], v[160:163], v[52:55], v[80:83]\n"
    "ds_read_b64_tr_b16 v[36:37], v10 offset:24576\n"
    "v_mfma_f32_16x16x32_bf16 v[84:87], v[164:167], v[52:55], v[84:87]\n"
    "ds_read_b64_tr_b16 v[38:39], v10 offset:26624\n"
    "v_mfma_f32_16x16x32_bf16 v[88:91], v[160:163], v[56:59], v[88:91]\n"
    "ds_read_b64_tr_b16 v[40:41], v11 offset:24576\n"
    "v_mfma_f32_16x16x32_bf16 v[92:95], v[164:167], v[56:59], v[92:95]\n"
    "ds_read_b64_tr_b16 v[42:43], v11 offset:26624\n"
    "s_waitcnt lgkmcnt(8)\n"
    "v_mfma_f32_16x16x32_bf16 v[64:67], v[168:171], v[112:115], v[64:67]\n"
    "ds_read_b64_tr_b16 v[44:45], v8 offset:28672\n"
    "v_mfma_f32_16x16x32_bf16 v[68:71], v[172:175], v[112:115], v[68:71]\n"
    "ds_read_b64_tr_b16 v[46:47], v8 offset:30720\n"
    "v_mfma_f32_16x16x32_bf16 v[72:75], v[168:171], v[116:119], v[72:75]\n"
    "ds_read_b64_tr_b16 v[48:49], v9 offset:28672\n"
    "v_mfma_f32_16x16x32_bf16 v[76:79], v[172:175], v[116:119], v[76:79]\n"
    "ds_read_b64_tr_b16 v[50:51], v9 offset:30720\n"
    "v_mfma_f32_16x16x32_bf16 v[80:83], v[168:171], v[120:123], v[80:83]\n"
    "ds_read_b64_tr_b16 v[52:53], v10 offset:28672\n"
    "v_mfma_f32_16x16x32_bf16 v[84:87], v[172:175], v[120:123], v[84:87]\n"
    "ds_read_b64_tr_b16 v[54:55], v10 offset:30720\n"
    "v_mfma_f32_16x16x32_bf16 v[88:91], v[168:171], v[124:127], v[88:91]\n"
    "ds_read_b64_tr_b16 v[56:57], v11 offset:28672\n"
    "v_mfma_f32_16x16x32_bf16 v[92:95], v[172:175], v[124:127], v[92:95]\n"
    "ds_read_b64_tr_b16 v[58:59], v11 offset:30720\n"
    "s_waitcnt lgkmcnt(8)\n"
    "s_barrier\n"
    "v_mfma_f32_16x16x32_bf16 v[64:67], v[176:179], v[28:31], v[64:67]\n"
    "ds_read_b64_tr_b16 v[112:113], v8 offset:32768\n"
    "v_mfma_f32_16x16x32_bf16 v[68:71], v[180:183], v[28:31], v[68:71]\n"
    "ds_read_b64_tr_b16 v[114:115], v8 offset:34816\n"
    "v_mfma_f32_16x16x32_bf16 v[72:75], v[176:179], v[32:35], v[72:75]\n"
    "ds_read_b64_tr_b16 v[116:117], v9 offset:32768\n"
    "v_mfma_f32_16x16x32_bf16 v[76:79], v[180:183], v[32:35], v[76:79]\n"
    "ds_read_b64_tr_b16 v[118:119], v9 offset:34816\n"
    "v_mfma_f32_16x16x32_bf16 v[80:83], v[176:179], v[36:39], v[80:83]\n"
    "ds_read_b64_tr_b16 v[120:121], v10 offset:32768\n"
    "v_mfma_f32_16x16x32_bf16 v[84:87], v[180:183], v[36:39], v[84:87]\n"
    "ds_read_b64_tr_b16 v[122:123], v10 offset:34816\n"
    "v_mfma_f32_16x16x32_bf16 v[88:91], v[176:179], v[40:43], v[88:91]\n"
    "ds_read_b64_tr_b16 v[124:125], v11 offset:32768\n"
    "v_mfma_f32_16x16x32_bf16 v[92:95], v[180:183], v[40:43], v[92:95]\n"
    "ds_read_b64_tr_b16 v[126:127], v11 offset:34816\n"
    "s_waitcnt lgkmcnt(8)\n"
    "v_mfma_f32_16x16x32_bf16 v[64:67], v[184:187], v[44:47], v[64:67]\n"
    "ds_read_b64_tr_b16 v[28:29], v8 offset:36864\n"
    "v_mfma_f32_16x16x32_bf16 v[68:71], v[188:191], v[44:47], v[68:71]\n"
    "ds_read_b64_tr_b16 v[30:31], v8 offset:38912\n"
    "v_mfma_f32_16x16x32_bf16 v[72:75], v[184:187], v[48:51], v[72:75]\n"
    "ds_read_b64_tr_b16 v[32:33], v9 offset:36864\n"
    "v_mfma_f32_16x16x32_bf16 v[76:79], v[188:191], v[48:51], v[76:79]\n"
    "ds_read_b64_tr_b16 v[34:35], v9 offset:38912\n"
    "v_mfma_f32_16x16x32_bf16 v[80:83], v[184:187], v[52:55], v[80:83]\n"
    "ds_read_b64_tr_b16 v[36:37], v10 offset:36864\n"
    "v_mfma_f32_16x16x32_bf16 v[84:87], v[188:191], v[52:55], v[84:87]\n"
    "ds_read_b64_tr_b16 v[38:39], v10 offset:38912\n"
    "v_mfma_f32_16x16x32_bf16 v[88:91], v[184:187], v[56:59], v[88:91]\n"
    "ds_read_b64_tr_b16 v[40:41], v11 offset:36864\n"
    "v_mfma_f32_16x16x32_bf16 v[92:95], v[188:191], v[56:59], v[92:95]\n"
    "ds_read_b64_tr_b16 v[42:43], v11 offset:38912\n"
    "s_waitcnt lgkmcnt(8)\n"
    "v_mfma_f32_16x16x32_bf16 v[64:67], v[192:195], v[112:115], v[64:67]\n"
    "ds_read_b64_tr_b16 v[44:45], v8 offset:40960\n"
    "v_mfma_f32_16x16x32_bf16 v[68:71], v[196:199], v[112:115], v[68:71]\n"
    "ds_read_b64_tr_b16 v[46:47], v8 offset:43008\n"
    "v_mfma_f32_16x16x32_bf16 v[72:75], v[192:195], v[116:119], v[72:75]\n"
    "ds_read_b64_tr_b16 v[48:49], v9 offset:40960\n"
    "v_mfma_f32_16x16x32_bf16 v[76:79], v[196:199], v[116:119], v[76:79]\n"
    "ds_read_b64_tr_b16 v[50:51], v9 offset:43008\n"
    "v_mfma_f32_16x16x32_bf16 v[80:83], v[192:195], v[120:123], v[80:83]\n"
    "ds_read_b64_tr_b16 v[52:53], v10 offset:40960\n"
    "v_mfma_f32_16x16x32_bf16 v[84:87], v[196:199], v[120:123], v[84:87]\n"
    "ds_read_b64_tr_b16 v[54:55], v10 offset:43008\n"
    "v_mfma_f32_16x16x32_bf16 v[88:91], v[192:195], v[124:127], v[88:91]\n"
    "ds_read_b64_tr_b16 v[56:57], v11 offset:40960\n"
    "v_mfma_f32_16x16x32_bf16 v[92:95], v[196:199], v[124:127], v[92:95]\n"
    "ds_read_b64_tr_b16 v[58:59], v11 offset:43008\n"
    "s_waitcnt lgkmcnt(8)\n"
    "v_mfma_f32_16x16x32_bf16 v[64:67], v[200:203], v[28:31], v[64:67]\n"
    "ds_read_b64_tr_b16 v[112:113], v8 offset:45056\n"
    "v_mfma_f32_16x16x32_bf16 v[68:71], v[204:207], v[28:31], v[68:71]\n"
    "ds_read_b64_tr_b16 v[114:115], v8 offset:47104\n"
    "v_mfma_f32_16x16x32_bf16 v[72:75], v[200:203], v[32:35], v[72:75]\n"
    "ds_read_b64_tr_b16 v[116:117], v9 offset:45056\n"
    "v_mfma_f32_16x16x32_bf16 v[76:79], v[204:207], v[32:35], v[76:79]\n"
    "ds_read_b64_tr_b16 v[118:119], v9 offset:47104\n"
    "v_mfma_f32_16x16x32_bf16 v[80:83], v[200:203], v[36:39], v[80:83]\n"
    "ds_read_b64_tr_b16 v[120:121], v10 offset:45056\n"
    "v_mfma_f32_16x16x32_bf16 v[84:87], v[204:207], v[36:39], v[84:87]\n"
    "ds_read_b64_tr_b16 v[122:123], v10 offset:47104\n"
    "v_mfma_f32_16x16x32_bf16 v[88:91], v[200:203], v[40:43], v[88:91]\n"
    "ds_read_b64_tr_b16 v[124:125], v11 offset:45056\n"
    "v_mfma_f32_16x16x32_bf16 v[92:95], v[204:207], v[40:43], v[92:95]\n"
    "ds_read_b64_tr_b16 v[126:127], v11 offset:47104\n"
    "s_waitcnt lgkmcnt(8)\n"
    "s_barrier\n"
    "v_mfma_f32_16x16x32_bf16 v[64:67], v[208:211], v[44:47], v[64:67]\n"
    "ds_read_b64_tr_b16 v[28:29], v8 offset:49152\n"
    "v_mfma_f32_16x16x32_bf16 v[68:71], v[212:215], v[44:47], v[68:71]\n"
    "ds_read_b64_tr_b16 v[30:31], v8 offset:51200\n"
    "v_mfma_f32_16x16x32_bf16 v[72:75], v[208:211], v[48:51], v[72:75]\n"
    "ds_read_b64_tr_b16 v[32:33], v9 offset:49152\n"
    "v_mfma_f32_16x16x32_bf16 v[76:79], v[212:215], v[48:51], v[76:79]\n"
    "ds_read_b64_tr_b16 v[34:35], v9 offset:51200\n"
    "v_mfma_f32_16x16x32_bf16 v[80:83], v[208:211], v[52:55], v[80:83]\n"
    "ds_read_b64_tr_b16 v[36:37], v10 offset:49152\n"
    "v_mfma_f32_16x16x32_bf16 v[84:87], v[212:215], v[52:55], v[84:87]\n"
    "ds_read_b64_tr_b16 v[38:39], v10 offset:51200\n"
    "v_mfma_f32_16x16x32_bf16 v[88:91], v[208:211], v[56:59], v[88:91]\n"
    "ds_read_b64_tr_b16 v[40:41], v11 offset:49152\n"
    "v_mfma_f32_16x16x32_bf16 v[92:95], v[212:215], v[56:59], v[92:95]\n"
    "ds_read_b64_tr_b16 v[42:43], v11 offset:51200\n"
    "s_waitcnt lgkmcnt(8)\n"
    "v_mfma_f32_16x16x32_bf16 v[64:67], v[216:219], v[112:115], v[64:67]\n"
    "ds_read_b64_tr_b16 v[44:45], v8 offset:53248\n"
    "v_mfma_f32_16x16x32_bf16 v[68:71], v[220:223], v[112:115], v[68:71]\n"
    "ds_read_b64_tr_b16 v[46:47], v8 offset:55296\n"
    "v_mfma_f32_16x16x32_bf16 v[72:75], v[216:219], v[116:119], v[72:75]\n"
    "ds_read_b64_tr_b16 v[48:49], v9 offset:53248\n"
    "v_mfma_f32_16x16x32_bf16 v[76:79], v[220:223], v[116:119], v[76:79]\n"
    "ds_read_b64_tr_b16 v[50:51], v9 offset:55296\n"
    "v_mfma_f32_16x16x32_bf16 v[80:83], v[216:219], v[120:123], v[80:83]\n"
    "ds_read_b64_tr_b16 v[52:53], v10 offset:53248\n"
    "v_mfma_f32_16x16x32_bf16 v[84:87], v[220:223], v[120:123], v[84:87]\n"
    "ds_read_b64_tr_b16 v[54:55], v10 offset:55296\n"
    "v_mfma_f32_16x16x32_bf16 v[88:91], v[216:219], v[124:127], v[88:91]\n"
    "ds_read_b64_tr_b16 v[56:57], v11 offset:53248\n"
    "v_mfma_f32_16x16x32_bf16 v[92:95], v[220:223], v[124:127], v[92:95]\n"
    "ds_read_b64_tr_b16 v[58:59], v11 offset:55296\n"
    "s_waitcnt lgkmcnt(8)\n"
    "v_mfma_f32_16x16x32_bf16 v[64:67], v[224:227], v[28:31], v[64:67]\n"
    "ds_read_b64_tr_b16 v[112:113], v8 offset:57344\n"
    "v_mfma_f32_16x16x32_bf16 v[68:71], v[228:231], v[28:31], v[68:71]\n"
    "ds_read_b64_tr_b16 v[114:115], v8 offset:59392\n"
    "v_mfma_f32_16x16x32_bf16 v[72:75], v[224:227], v[32:35], v[72:75]\n"
    "ds_read_b64_tr_b16 v[116:117], v9 offset:57344\n"
    "v_mfma_f32_16x16x32_bf16 v[76:79], v[228:231], v[32:35], v[76:79]\n"
    "ds_read_b64_tr_b16 v[118:119], v9 offset:59392\n"
    "v_mfma_f32_16x16x32_bf16 v[80:83], v[224:227], v[36:39], v[80:83]\n"
    "ds_read_b64_tr_b16 v[120:121], v10 offset:57344\n"
    "v_mfma_f32_16x16x32_bf16 v[84:87], v[228:231], v[36:39], v[84:87]\n"
    "ds_read_b64_tr_b16 v[122:123], v10 offset:59392\n"
    "v_mfma_f32_16x16x32_bf16 v[88:91], v[224:227], v[40:43], v[88:91]\n"
    "ds_read_b64_tr_b16 v[124:125], v11 offset:57344\n"
    "v_mfma_f32_16x16x32_bf16 v[92:95], v[228:231], v[40:43], v[92:95]\n"
    "ds_read_b64_tr_b16 v[126:127], v11 offset:59392\n"
    "s_waitcnt lgkmcnt(8)\n"
    "v_mfma_f32_16x16x32_bf16 v[64:67], v[232:235], v[44:47], v[64:67]\n"
    "ds_read_b64_tr_b16 v[28:29], v8 offset:61440\n"
    "v_mfma_f32_16x16x32_bf16 v[68:71], v[236:239], v[44:47], v[68:71]\n"
    "ds_read_b64_tr_b16 v[30:31], v8 offset:63488\n"
    "v_mfma_f32_16x16x32_bf16 v[72:75], v[232:235], v[48:51], v[72:75]\n"
    "ds_read_b64_tr_b16 v[32:33], v9 offset:61440\n"
    "v_mfma_f32_16x16x32_bf16 v[76:79], v[236:239], v[48:51], v[76:79]\n"
    "ds_read_b64_tr_b16 v[34:35], v9 offset:63488\n"
    "v_mfma_f32_16x16x32_bf16 v[80:83], v[232:235], v[52:55], v[80:83]\n"
    "ds_read_b64_tr_b16 v[36:37], v10 offset:61440\n"
    "v_mfma_f32_16x16x32_bf16 v[84:87], v[236:239], v[52:55], v[84:87]\n"
    "ds_read_b64_tr_b16 v[38:39], v10 offset:63488\n"
    "v_mfma_f32_16x16x32_bf16 v[88:91], v[232:235], v[56:59], v[88:91]\n"
    "ds_read_b64_tr_b16 v[40:41], v11 offset:61440\n"
    "v_mfma_f32_16x16x32_bf16 v[92:95], v[236:239], v[56:59], v[92:95]\n"
    "ds_read_b64_tr_b16 v[42:43], v11 offset:63488\n"
    "s_waitcnt lgkmcnt(0)\n"
    "s_barrier\n"
    "buffer_load_dwordx4 v[4:7], v3, s[28:31], s58 offen sc1\n"
    "buffer_load_dwordx4 v[8:11], v3, s[28:31], s58 offen offset:256 sc1\n"
    "v_mfma_f32_16x16x32_bf16 v[64:67], v[240:243], v[112:115], v[64:67]\n"
    "ds_read_b64_tr_b16 v[44:45], v12 offset:0\n"
    "v_mfma_f32_16x16x32_bf16 v[68:71], v[244:247], v[112:115], v[68:71]\n"
    "ds_read_b64_tr_b16 v[46:47], v12 offset:2048\n"
    "v_mfma_f32_16x16x32_bf16 v[72:75], v[240:243], v[116:119], v[72:75]\n"
    "ds_read_b64_tr_b16 v[48:49], v13 offset:0\n"
    "v_mfma_f32_16x16x32_bf16 v[76:79], v[244:247], v[116:119], v[76:79]\n"
    "ds_read_b64_tr_b16 v[50:51], v13 offset:2048\n"
    "v_mfma_f32_16x16x32_bf16 v[80:83], v[240:243], v[120:123], v[80:83]\n"
    "ds_read_b64_tr_b16 v[52:53], v12 offset:4096\n"
    "v_mfma_f32_16x16x32_bf16 v[84:87], v[244:247], v[120:123], v[84:87]\n"
    "ds_read_b64_tr_b16 v[54:55], v12 offset:6144\n"
    "v_mfma_f32_16x16x32_bf16 v[88:91], v[240:243], v[124:127], v[88:91]\n"
    "ds_read_b64_tr_b16 v[56:57], v13 offset:4096\n"
    "v_mfma_f32_16x16x32_bf16 v[92:95], v[244:247], v[124:127], v[92:95]\n"
    "ds_read_b64_tr_b16 v[58:59], v13 offset:6144\n"
    "s_waitcnt lgkmcnt(8)\n"
    "v_mfma_f32_16x16x32_bf16 v[64:67], v[248:251], v[28:31], v[64:67]\n"
    "ds_read_b64_tr_b16 v[112:113], v12 offset:8192\n"
    "v_mfma_f32_16x16x32_bf16 v[68:71], v[252:255], v[28:31], v[68:71]\n"
    "ds_read_b64_tr_b16 v[114:115], v12 offset:10240\n"
    "v_mfma_f32_16x16x32_bf16 v[72:75], v[248:251], v[32:35], v[72:75]\n"
    "ds_read_b64_tr_b16 v[116:117], v13 offset:8192\n"
    "v_mfma_f32_16x16x32_bf16 v[76:79], v[252:255], v[32:35], v[76:79]\n"
    "ds_read_b64_tr_b16 v[118:119], v13 offset:10240\n"
    "v_mfma_f32_16x16x32_bf16 v[80:83], v[248:251], v[36:39], v[80:83]\n"
    "ds_read_b64_tr_b16 v[120:121], v12 offset:12288\n"
    "v_mfma_f32_16x16x32_bf16 v[84:87], v[252:255], v[36:39], v[84:87]\n"
    "ds_read_b64_tr_b16 v[122:123], v12 offset:14336\n"
    "v_mfma_f32_16x16x32_bf16 v[88:91], v[248:251], v[40:43], v[88:91]\n"
    "ds_read_b64_tr_b16 v[124:125], v13 offset:12288\n"
    "v_mfma_f32_16x16x32_bf16 v[92:95], v[252:255], v[40:43], v[92:95]\n"
    "ds_read_b64_tr_b16 v[126:127], v13 offset:14336\n"
    "s_waitcnt lgkmcnt(8)\n"
    "v_mfma_f32_16x16x32_bf16 v[96:99], v[128:131], v[44:47], 0\n"
    "ds_read_b64_tr_b16 v[28:29], v12 offset:16384\n"
    "v_mfma_f32_16x16x32_bf16 v[100:103], v[132:135], v[44:47], 0\n"
    "ds_read_b64_tr_b16 v[30:31], v12 offset:18432\n"
    "v_mfma_f32_16x16x32_bf16 v[104:107], v[128:131], v[48:51], 0\n"
    "ds_read_b64_tr_b16 v[32:33], v13 offset:16384\n"
    "v_mfma_f32_16x16x32_bf16 v[108:111], v[132:135], v[48:51], 0\n"
    "ds_read_b64_tr_b16 v[34:35], v13 offset:18432\n"
    "v_mfma_f32_16x16x32_bf16 v[96:99], v[136:139], v[52:55], v[96:99]\n"
    "ds_read_b64_tr_b16 v[36:37], v12 offset:20480\n"
    "v_mfma_f32_16x16x32_bf16 v[100:103], v[140:143], v[52:55], v[100:103]\n"
    "ds_read_b64_tr_b16 v[38:39], v12 offset:22528\n"
    "v_mfma_f32_16x16x32_bf16 v[104:107], v[136:139], v[56:59], v[104:107]\n"
    "ds_read_b64_tr_b16 v[40:41], v13 offset:20480\n"
    "v_mfma_f32_16x16x32_bf16 v[108:111], v[140:143], v[56:59], v[108:111]\n"
    "ds_read_b64_tr_b16 v[42:43], v13 offset:22528\n"
    "s_waitcnt lgkmcnt(8)\n"
    "s_waitcnt vmcnt(0)\n"
    "v_max3_u32 v60, v4, v5, v6\n"
    "v_max3_u32 v60, v60, v7, v8\n"
    "v_max3_u32 v60, v60, v9, v10\n"
    "v_max_u32 v60, v60, v11\n"
    "v_cmp_eq_u32 vcc, -1, v60\n"
    "s_cbranch_vccnz .Lh3_slow_8\n"
    ".Lh3_back_9:\n"
    "v_mfma_f32_16x16x32_bf16 v[96:99], v[144:147], v[112:115], v[96:99]\n"
    "ds_read_b64_tr_b16 v[44:45], v12 offset:24576\n"
    "v_fma_f32 v64, -v4, v20, v64\n"
    "v_fma_f32 v65, -v4, v21, v65\n"
    "v_mfma_f32_16x16x32_bf16 v[100:103], v[148:151], v[112:115], v[100:103]\n"
    "ds_read_b64_tr_b16 v[46:47], v12 offset:26624\n"
    "v_fma_f32 v66, -v4, v22, v66\n"
    "v_fma_f32 v67, -v4, v23, v67\n"
    "v_mfma_f32_16x16x32_bf16 v[104:107], v[144:147], v[116:119], v[104:107]\n"
    "ds_read_b64_tr_b16 v[48:49], v13 offset:24576\n"
    "v_fma_f32 v68, -v4, v24, v68\n"
    "v_fma_f32 v69, -v4, v25, v69\n"
    "v_mfma_f32_16x16x32_bf16 v[108:111], v[148:151], v[116:119], v[108:111]\n"
    "ds_read_b64_tr_b16 v[50:51], v13 offset:26624\n"
    "v_fma_f32 v70, -v4, v26, v70\n"
    "v_fma_f32 v71, -v4, v27, v71\n"
    "v_mfma_f32_16x16x32_bf16 v[96:99], v[152:155], v[120:123], v[96:99]\n"
    "ds_read_b64_tr_b16 v[52:53], v12 offset:28672\n"
    "v_cvt_pk_bf16_f32 v60, v64, v65\n"
    "v_cvt_pk_bf16_f32 v61, v66, v67\n"
    "v_mfma_f32_16x16x32_bf16 v[100:103], v[156:159], v[120:123], v[100:103]\n"
    "ds_read_b64_tr_b16 v[54:55], v12 offset:30720\n"
    "v_cvt_pk_bf16_f32 v62, v68, v69\n"
    "v_cvt_pk_bf16_f32 v63, v70, v71\n"
    "v_mfma_f32_16x16x32_bf16 v[104:107], v[152:155], v[124:127], v[104:107]\n"
    "ds_read_b64_tr_b16 v[56:57], v13 offset:28672\n"
    "v_pk_max_i16 v60, v60, 0\n"
    "v_pk_max_i16 v61, v61, 0\n"
    "v_mfma_f32_16x16x32_bf16 v[108:111], v[156:159], v[124:127], v[108:111]\n"
    "ds_read_b64_tr_b16 v[58:59], v13 offset:30720\n"
    "v_pk_max_i16 v62, v62, 0\n"
    "v_pk_max_i16 v63, v63, 0\n"
    "s_waitcnt lgkmcnt(8)\n"
    "s_barrier\n"
    "v_mfma_f32_16x16x32_bf16 v[96:99], v[160:163], v[28:31], v[96:99]\n"
    "ds_read_b64_tr_b16 v[112:113], v12 offset:32768\n"
    "buffer_store_dwordx4 v[60:63], v2, s[28:31], s56 offen offset:0 sc1\n"
    "v_fma_f32 v72, -v5, v20, v72\n"
    "v_mfma_f32_16x16x32_bf16 v[100:103], v[164:167], v[28:31], v[100:103]\n"
    "ds_read_b64_tr_b16 v[114:115], v12 offset:34816\n"
    "v_fma_f32 v73, -v5, v21, v73\n"
    "v_fma_f32 v74, -v5, v22, v74\n"
    "v_mfma_f32_16x16x32_bf16 v[104:107], v[160:163], v[32:35], v[104:107]\n"
    "ds_read_b64_tr_b16 v[116:117], v13 offset:32768\n"
    "v_fma_f32 v75, -v5, v23, v75\n"
    "v_fma_f32 v76, -v5, v24, v76\n"
    "v_mfma_f32_16x16x32_bf16 v[108:111], v[164:167], v[32:35], v[108:111]\n"
    "ds_read_b64_tr_b16 v[118:119], v13 offset:34816\n"
    "v_fma_f32 v77, -v5, v25, v77\n"
    "v_fma_f32 v78, -v5, v26, v78\n"
    "v_mfma_f32_16x16x32_bf16 v[96:99], v[168:171], v[36:39], v[96:99]\n"
    "ds_read_b64_tr_b16 v[120:121], v12 offset:36864\n"
    "v_fma_f32 v79, -v5, v27, v79\n"
    "v_cvt_pk_bf16_f32 v16, v72, v73\n"
    "v_mfma_f32_16x16x32_bf16 v[100:103], v[172:175], v[36:39], v[100:103]\n"
    "ds_read_b64_tr_b16 v[122:123], v12 offset:38912\n"
    "v_cvt_pk_bf16_f32 v17, v74, v75\n"
    "v_cvt_pk_bf16_f32 v18, v76, v77\n"
    "v_mfma_f32_16x16x32_bf16 v[104:107], v[168:171], v[40:43], v[104:107]\n"
    "ds_read_b64_tr_b16 v[124:125], v13 offset:36864\n"
    "v_cvt_pk_bf16_f32 v19, v78, v79\n"
    "v_pk_max_i16 v16, v16, 0\n"
    "v_mfma_f32_16x16x32_bf16 v[108:111], v[172:175], v[40:43], v[108:111]\n"
    "ds_read_b64_tr_b16 v[126:127], v13 offset:38912\n"
    "v_pk_max_i16 v17, v17, 0\n"
    "v_pk_max_i16 v18, v18, 0\n"
    "s_waitcnt lgkmcnt(8)\n"
    "v_mfma_f32_16x16x32_bf16 v[96:99], v[176:179], v[44:47], v[96:99]\n"
    "ds_read_b64_tr_b16 v[28:29], v12 offset:40960\n"
    "v_pk_max_i16 v19, v19, 0\n"
    "buffer_store_dwordx4 v[16:19], v2, s[28:31], s56 offen offset:1024 sc1\n"
    "v_mfma_f32_16x16x32_bf16 v[100:103], v[180:183], v[44:47], v[100:103]\n"
    "ds_read_b64_tr_b16 v[30:31], v12 offset:43008\n"
    "s_add_u32 s56, s56, 114688\n"
    "v_fma_f32 v80, -v6, v20, v80\n"
    "v_mfma_f32_16x16x32_bf16 v[104:107], v[176:179], v[48:51], v[104:107]\n"
    "ds_read_b64_tr_b16 v[32:33], v13 offset:40960\n"
    "v_fma_f32 v81, -v6, v21, v81\n"
    "v_fma_f32 v82, -v6, v22, v82\n"
    "v_mfma_f32_16x16x32_bf16 v[108:111], v[180:183], v[48:51], v[108:111]\n"
    "ds_read_b64_tr_b16 v[34:35], v13 offset:43008\n"
    "v_fma_f32 v83, -v6, v23, v83\n"
    "v_fma_f32 v84, -v6, v24, v84\n"
    "v_mfma_f32_16x16x32_bf16 v[96:99], v[184:187], v[52:55], v[96:99]\n"
    "ds_read_b64_tr_b16 v[36:37], v12 offset:45056\n"
    "v_fma_f32 v85, -v6, v25, v85\n"
    "v_fma_f32 v86, -v6, v26, v86\n"
    "v_mfma_f32_16x16x32_bf16 v[100:103], v[188:191], v[52:55], v[100:103]\n"
    "ds_read_b64_tr_b16 v[38:39], v12 offset:47104\n"
    "v_fma_f32 v87, -v6, v27, v87\n"
    "v_cvt_pk_bf16_f32 v60, v80, v81\n"
    "v_mfma_f32_16x16x32_bf16 v[104:107], v[184:187], v[56:59], v[104:107]\n"
    "ds_read_b64_tr_b16 v[40:41], v13 offset:45056\n"
    "v_cvt_pk_bf16_f32 v61, v82, v83\n"
    "v_cvt_pk_bf16_f32 v62, v84, v85\n"
    "v_mfma_f32_16x16x32_bf16 v[108:111], v[188:191], v[56:59], v[108:111]\n"
    "ds_read_b64_tr_b16 v[42:43], v13 offset:47104\n"
    "v_cvt_pk_bf16_f32 v63, v86, v87\n"
    "v_pk_max_i16 v60, v60, 0\n"
    "s_waitcnt lgkmcnt(8)\n"
    "v_mfma_f32_16x16x32_bf16 v[96:99], v[192:195], v[112:115], v[96:99]\n"
    "ds_read_b64_tr_b16 v[44:45], v12 offset:49152\n"
    "v_pk_max_i16 v61, v61, 0\n"
    "v_pk_max_i16 v62, v62, 0\n"
    "v_mfma_f32_16x16x32_bf16 v[100:103], v[196:199], v[112:115], v[100:103]\n"
    "ds_read_b64_tr_b16 v[46:47], v12 offset:51200\n"
    "v_pk_max_i16 v63, v63, 0\n"
    "buffer_store_dwordx4 v[60:63], v2, s[28:31], s56 offen offset:0 sc1\n"
    "v_mfma_f32_16x16x32_bf16 v[104:107], v[192:195], v[116:119], v[104:107]\n"
    "ds_read_b64_tr_b16 v[48:49], v13 offset:49152\n"
    "v_fma_f32 v88, -v7, v20, v88\n"
    "v_fma_f32 v89, -v7, v21, v89\n"
    "v_mfma_f32_16x16x32_bf16 v[108:111], v[196:199], v[116:119], v[108:111]\n"
    "ds_read_b64_tr_b16 v[50:51], v13 offset:51200\n"
    "v_fma_f32 v90, -v7, v22, v90\n"
    "v_fma_f32 v91, -v7, v23, v91\n"
    "v_mfma_f32_16x16x32_bf16 v[96:99], v[200:203], v[120:123], v[96:99]\n"
    "ds_read_b64_tr_b16 v[52:53], v12 offset:53248\n"
    "v_fma_f32 v92, -v7, v24, v92\n"
    "v_fma_f32 v93, -v7, v25, v93\n"
    "v_mfma_f32_16x16x32_bf16 v[100:103], v[204:207], v[120:123], v[100:103]\n"
    "ds_read_b64_tr_b16 v[54:55], v12 offset:55296\n"
    "v_fma_f32 v94, -v7, v26, v94\n"
    "v_fma_f32 v95, -v7, v27, v95\n"
    "v_mfma_f32_16x16x32_bf16 v[104:107], v[200:203], v[124:127], v[104:107]\n"
    "ds_read_b64_tr_b16 v[56:57], v13 offset:53248\n"
    "v_cvt_pk_bf16_f32 v16, v88, v89\n"
    "v_cvt_pk_bf16_f32 v17, v90, v91\n"
    "v_mfma_f32_16x16x32_bf16 v[108:111], v[204:207], v[124:127], v[108:111]\n"
    "ds_read_b64_tr_b16 v[58:59], v13 offset:55296\n"
    "v_cvt_pk_bf16_f32 v18, v92, v93\n"
    "v_cvt_pk_bf16_f32 v19, v94, v95\n"
    "s_waitcnt lgkmcnt(8)\n"
    "v_mfma_f32_16x16x32_bf16 v[96:99], v[208:211], v[28:31], v[96:99]\n"
    "ds_read_b64_tr_b16 v[112:113], v12 offset:57344\n"
    "v_pk_max_i16 v16, v16, 0\n"
    "v_pk_max_i16 v17, v17, 0\n"
    "v_mfma_f32_16x16x32_bf16 v[100:103], v[212:215], v[28:31], v[100:103]\n"
    "ds_read_b64_tr_b16 v[114:115], v12 offset:59392\n"
    "v_pk_max_i16 v18, v18, 0\n"
    "v_pk_max_i16 v19, v19, 0\n"
    "v_mfma_f32_16x16x32_bf16 v[104:107], v[208:211], v[32:35], v[104:107]\n"
    "ds_read_b64_tr_b16 v[116:117], v13 offset:57344\n"
    "buffer_store_dwordx4 v[16:19], v2, s[28:31], s56 offen offset:1024 sc1\n"
    "s_add_u32 s56, s56, 114688\n"
    "v_mfma_f32_16x16x32_bf16 v[108:111], v[212:215], v[32:35], v[108:111]\n"
    "ds_read_b64_tr_b16 v[118:119], v13 offset:59392\n"
    "v_mfma_f32_16x16x32_bf16 v[96:99], v[216:219], v[36:39], v[96:99]\n"
    "ds_read_b64_tr_b16 v[120:121], v12 offset:61440\n"
    "v_mfma_f32_16x16x32_bf16 v[100:103], v[220:223], v[36:39], v[100:103]\n"
    "ds_read_b64_tr_b16 v[122:123], v12 offset:63488\n"
    "v_mfma_f32_16x16x32_bf16 v[104:107], v[216:219], v[40:43], v[104:107]\n"
    "ds_read_b64_tr_b16 v[124:125], v13 offset:61440\n"
    "v_mfma_f32_16x16x32_bf16 v[108:111], v[220:223], v[40:43], v[108:111]\n"
    "ds_read_b64_tr_b16 v[126:127], v13 offset:63488\n"
    "s_waitcnt lgkmcnt(8)\n"
    "v_mfma_f32_16x16x32_bf16 v[96:99], v[224:227], v[44:47], v[96:99]\n"
    "ds_read_b64_tr_b16 v[28:29], v14 offset:0\n"
    "v_mfma_f32_16x16x32_bf16 v[100:103], v[228:231], v[44:47], v[100:103]\n"
    "ds_read_b64_tr_b16 v[30:31], v14 offset:2048\n"
    "v_mfma_f32_16x16x32_bf16 v[104:107], v[224:227], v[48:51], v[104:107]\n"
    "ds_read_b64_tr_b16 v[32:33], v15 offset:0\n"
    "v_mfma_f32_16x16x32_bf16 v[108:111], v[228:231], v[48:51], v[108:111]\n"
    "ds_read_b64_tr_b16 v[34:35], v15 offset:2048\n"
    "v_mfma_f32_16x16x32_bf16 v[96:99], v[232:235], v[52:55], v[96:99]\n"
    "ds_read_b64_tr_b16 v[36:37], v14 offset:4096\n"
    "v_mfma_f32_16x16x32_bf16 v[100:103], v[236:239], v[52:55], v[100:103]\n"
    "ds_read_b64_tr_b16 v[38:39], v14 offset:6144\n"
    "v_mfma_f32_16x16x32_bf16 v[104:107], v[232:235], v[56:59], v[104:107]\n"
    "ds_read_b64_tr_b16 v[40:41], v15 offset:4096\n"
    "v_mfma_f32_16x16x32_bf16 v[108:111], v[236:239], v[56:59], v[108:111]\n"
    "ds_read_b64_tr_b16 v[42:43], v15 offset:6144\n"
    "s_waitcnt lgkmcnt(8)\n"
    "v_mfma_f32_16x16x32_bf16 v[96:99], v[240:243], v[112:115], v[96:99]\n"
    "ds_read_b64_tr_b16 v[44:45], v14 offset:8192\n"
    "v_mfma_f32_16x16x32_bf16 v[100:103], v[244:247], v[112:115], v[100:103]\n"
    "ds_read_b64_tr_b16 v[46:47], v14 offset:10240\n"
    "v_mfma_f32_16x16x32_bf16 v[104:107], v[240:243], v[116:119], v[104:107]\n"
    "ds_read_b64_tr_b16 v[48:49], v15 offset:8192\n"
    "v_mfma_f32_16x16x32_bf16 v[108:111], v[244:247], v[116:119], v[108:111]\n"
    "ds_read_b64_tr_b16 v[50:51], v15 offset:10240\n"
    "v_mfma_f32_16x16x32_bf16 v[96:99], v[248:251], v[120:123], v[96:99]\n"
    "ds_read_b64_tr_b16 v[52:53], v14 offset:12288\n"
    "v_mfma_f32_16x16x32_bf16 v[100:103], v[252:255], v[120:123], v[100:103]\n"
    "ds_read_b64_tr_b16 v[54:55], v14 offset:14336\n"
    "v_mfma_f32_16x16x32_bf16 v[104:107], v[248:251], v[124:127], v[104:107]\n"
    "ds_read_b64_tr_b16 v[56:57], v15 offset:12288\n"
    "v_mfma_f32_16x16x32_bf16 v[108:111], v[252:255], v[124:127], v[108:111]\n"
    "ds_read_b64_tr_b16 v[58:59], v15 offset:14336\n"
    "s_waitcnt lgkmcnt(8)\n"
    "v_mfma_f32_16x16x32_bf16 v[112:115], v[128:131], v[28:31], 0\n"
    "ds_read_b64_tr_b16 v[64:65], v14 offset:16384\n"
    "v_fma_f32 v96, -v8, v20, v96\n"
    "v_fma_f32 v97, -v8, v21, v97\n"
    "v_mfma_f32_16x16x32_bf16 v[116:119], v[132:135], v[28:31], 0\n"
    "ds_read_b64_tr_b16 v[66:67], v14 offset:18432\n"
    "v_fma_f32 v98, -v8, v22, v98\n"
    "v_fma_f32 v99, -v8, v23, v99\n"
    "v_mfma_f32_16x16x32_bf16 v[120:123], v[128:131], v[32:35], 0\n"
    "ds_read_b64_tr_b16 v[68:69], v15 offset:16384\n"
    "v_fma_f32 v100, -v8, v24, v100\n"
    "v_fma_f32 v101, -v8, v25, v101\n"
    "v_mfma_f32_16x16x32_bf16 v[124:127], v[132:135], v[32:35], 0\n"
    "ds_read_b64_tr_b16 v[70:71], v15 offset:18432\n"
    "v_fma_f32 v102, -v8, v26, v102\n"
    "v_fma_f32 v103, -v8, v27, v103\n"
    "v_mfma_f32_16x16x32_bf16 v[112:115], v[136:139], v[36:39], v[112:115]\n"
    "ds_read_b64_tr_b16 v[72:73], v14 offset:20480\n"
    "v_cvt_pk_bf16_f32 v60, v96, v97\n"
    "v_cvt_pk_bf16_f32 v61, v98, v99\n"
    "v_mfma_f32_16x16x32_bf16 v[116:119], v[140:143], v[36:39], v[116:119]\n"
    "ds_read_b64_tr_b16 v[74:75], v14 offset:22528\n"
    "v_cvt_pk_bf16_f32 v62, v100, v101\n"
    "v_cvt_pk_bf16_f32 v63, v102, v103\n"
    "v_mfma_f32_16x16x32_bf16 v[120:123], v[136:139], v[40:43], v[120:123]\n"
    "ds_read_b64_tr_b16 v[76:77], v15 offset:20480\n"
    "v_pk_max_i16 v60, v60, 0\n"
    "v_pk_max_i16 v61, v61, 0\n"
    "v_mfma_f32_16x16x32_bf16 v[124:127], v[140:143], v[40:43], v[124:127]\n"
    "ds_read_b64_tr_b16 v[78:79], v15 offset:22528\n"
    "v_pk_max_i16 v62, v62, 0\n"
    "v_pk_max_i16 v63, v63, 0\n"
    "s_waitcnt lgkmcnt(8)\n"
    "v_mfma_f32_16x16x32_bf16 v[112:115], v[144:147], v[44:47], v[112:115]\n"
    "ds_read_b64_tr_b16 v[28:29], v14 offset:24576\n"
    "buffer_store_dwordx4 v[60:63], v2, s[28:31], s56 offen offset:0 sc1\n"
    "v_fma_f32 v104, -v9, v20, v104\n"
    "v_mfma_f32_16x16x32_bf16 v[116:119], v[148:151], v[44:47], v[116:119]\n"
    "ds_read_b64_tr_b16 v[30:31], v14 offset:26624\n"
    "v_fma_f32 v105, -v9, v21, v105\n"
    "v_fma_f32 v106, -v9, v22, v106\n"
    "v_mfma_f32_16x16x32_bf16 v[120:123], v[144:147], v[48:51], v[120:123]\n"
    "ds_read_b64_tr_b16 v[32:33], v15 offset:24576\n"
    "v_fma_f32 v107, -v9, v23, v107\n"
    "v_fma_f32 v108, -v9, v24, v108\n"
    "v_mfma_f32_16x16x32_bf16 v[124:127], v[148:151], v[48:51], v[124:127]\n"
    "ds_read_b64_tr_b16 v[34:35], v15 offset:26624\n"
    "v_fma_f32 v109, -v9, v25, v109\n"
    "v_fma_f32 v110, -v9, v26, v110\n"
    "v_mfma_f32_16x16x32_bf16 v[112:115], v[152:155], v[52:55], v[112:115]\n"
    "ds_read_b64_tr_b16 v[36:37], v14 offset:28672\n"
    "v_fma_f32 v111, -v9, v27, v111\n"
    "v_cvt_pk_bf16_f32 v16, v104, v105\n"
    "v_mfma_f32_16x16x32_bf16 v[116:119], v[156:159], v[52:55], v[116:119]\n"
    "ds_read_b64_tr_b16 v[38:39], v14 offset:30720\n"
    "v_cvt_pk_bf16_f32 v17, v106, v107\n"
    "v_cvt_pk_bf16_f32 v18, v108, v109\n"
    "v_mfma_f32_16x16x32_bf16 v[120:123], v[152:155], v[56:59], v[120:123]\n"
    "ds_read_b64_tr_b16 v[40:41], v15 offset:28672\n"
    "v_cvt_pk_bf16_f32 v19, v110, v111\n"
    "v_pk_max_i16 v16, v16, 0\n"
    "v_mfma_f32_16x16x32_bf16 v[124:127], v[156:159], v[56:59], v[124:127]\n"
    "ds_read_b64_tr_b16 v[42:43], v15 offset:30720\n"
    "v_pk_max_i16 v17, v17, 0\n"
    "v_pk_max_i16 v18, v18, 0\n"
    "s_waitcnt lgkmcnt(8)\n"
    "v_mfma_f32_16x16x32_bf16 v[112:115], v[160:163], v[64:67], v[112:115]\n"
    "ds_read_b64_tr_b16 v[44:45], v14 offset:32768\n"
    "v_pk_max_i16 v19, v19, 0\n"
    "buffer_store_dwordx4 v[16:19], v2, s[28:31], s56 offen offset:1024 sc1\n"
    "v_mfma_f32_16x16x32_bf16 v[116:119], v[164:167], v[64:67], v[116:119]\n"
    "ds_read_b64_tr_b16 v[46:47], v14 offset:34816\n"
    "s_add_u32 s56, s56, 114688\n"
    "v_mfma_f32_16x16x32_bf16 v[120:123], v[160:163], v[68:71], v[120:123]\n"
    "ds_read_b64_tr_b16 v[48:49], v15 offset:32768\n"
    "v_mfma_f32_16x16x32_bf16 v[124:127], v[164:167], v[68:71], v[124:127]\n"
    "ds_read_b64_tr_b16 v[50:51], v15 offset:34816\n"
    "v_mfma_f32_16x16x32_bf16 v[112:115], v[168:171], v[72:75], v[112:115]\n"
    "ds_read_b64_tr_b16 v[52:53], v14 offset:36864\n"
    "v_mfma_f32_16x16x32_bf16 v[116:119], v[172:175], v[72:75], v[116:119]\n"
    "ds_read_b64_tr_b16 v[54:55], v14 offset:38912\n"
    "v_mfma_f32_16x16x32_bf16 v[120:123], v[168:171], v[76:79], v[120:123]\n"
    "ds_read_b64_tr_b16 v[56:57], v15 offset:36864\n"
    "v_mfma_f32_16x16x32_bf16 v[124:127], v[172:175], v[76:79], v[124:127]\n"
    "ds_read_b64_tr_b16 v[58:59], v15 offset:38912\n"
    "s_waitcnt lgkmcnt(8)\n"
    "v_mfma_f32_16x16x32_bf16 v[112:115], v[176:179], v[28:31], v[112:115]\n"
    "ds_read_b64_tr_b16 v[64:65], v14 offset:40960\n"
    "v_mfma_f32_16x16x32_bf16 v[116:119], v[180:183], v[28:31], v[116:119]\n"
    "ds_read_b64_tr_b16 v[66:67], v14 offset:43008\n"
    "v_mfma_f32_16x16x32_bf16 v[120:123], v[176:179], v[32:35], v[120:123]\n"
    "ds_read_b64_tr_b16 v[68:69], v15 offset:40960\n"
    "v_mfma_f32_16x16x32_bf16 v[124:127], v[180:183], v[32:35], v[124:127]\n"
    "ds_read_b64_tr_b16 v[70:71], v15 offset:43008\n"
    "v_mfma_f32_16x16x32_bf16 v[112:115], v[184:187], v[36:39], v[112:115]\n"
    "ds_read_b64_tr_b16 v[72:73], v14 offset:45056\n"
    "v_mfma_f32_16x16x32_bf16 v[116:119], v[188:191], v[36:39], v[116:119]\n"
    "ds_read_b64_tr_b16 v[74:75], v14 offset:47104\n"
    "v_mfma_f32_16x16x32_bf16 v[120:123], v[184:187], v[40:43], v[120:123]\n"
    "ds_read_b64_tr_b16 v[76:77], v15 offset:45056\n"
    "v_mfma_f32_16x16x32_bf16 v[124:127], v[188:191], v[40:43], v[124:127]\n"
    "ds_read_b64_tr_b16 v[78:79], v15 offset:47104\n"
    "s_waitcnt lgkmcnt(8)\n"
    "v_mov_b32 v4, s64\n"
    "v_mov_b32 v5, s65\n"
    "s_waitcnt vmcnt(2)\n"
    "s_mov_b64 exec, 1\n"
    "buffer_store_dwordx2 v[4:5], off, s[28:31], s59 sc1\n"
    "s_mov_b64 exec, -1\n"
    "s_add_u32 s59, s59, 114688\n"
    "v_mfma_f32_16x16x32_bf16 v[112:115], v[192:195], v[44:47], v[112:115]\n"
    "ds_read_b64_tr_b16 v[28:29], v14 offset:49152\n"
    "v_mfma_f32_16x16x32_bf16 v[116:119], v[196:199], v[44:47], v[116:119]\n"
    "ds_read_b64_tr_b16 v[30:31], v14 offset:51200\n"
    "v_mfma_f32_16x16x32_bf16 v[120:123], v[192:195], v[48:51], v[120:123]\n"
    "ds_read_b64_tr_b16 v[32:33], v15 offset:49152\n"
    "v_mfma_f32_16x16x32_bf16 v[124:127], v[196:199], v[48:51], v[124:127]\n"
    "ds_read_b64_tr_b16 v[34:35], v15 offset:51200\n"
    "v_mfma_f32_16x16x32_bf16 v[112:115], v[200:203], v[52:55], v[112:115]\n"
    "ds_read_b64_tr_b16 v[36:37], v14 offset:53248\n"
    "v_mfma_f32_16x16x32_bf16 v[116:119], v[204:207], v[52:55], v[116:119]\n"
    "ds_read_b64_tr_b16 v[38:39], v14 offset:55296\n"
    "v_mfma_f32_16x16x32_bf16 v[120:123], v[200:203], v[56:59], v[120:123]\n"
    "ds_read_b64_tr_b16 v[40:41], v15 offset:53248\n"
    "v_mfma_f32_16x16x32_bf16 v[124:127], v[204:207], v[56:59], v[124:127]\n"
    "ds_read_b64_tr_b16 v[42:43], v15 offset:55296\n"
    "s_waitcnt lgkmcnt(8)\n"
    "v_mfma_f32_16x16x32_bf16 v[112:115], v[208:211], v[64:67], v[112:115]\n"
    "ds_read_b64_tr_b16 v[44:45], v14 offset:57344\n"
    "v_mfma_f32_16x16x32_bf16 v[116:119], v[212:215], v[64:67], v[116:119]\n"
    "ds_read_b64_tr_b16 v[46:47], v14 offset:59392\n"
    "v_mfma_f32_16x16x32_bf16 v[120:123], v[208:211], v[68:71], v[120:123]\n"
    "ds_read_b64_tr_b16 v[48:49], v15 offset:57344\n"
    "v_mfma_f32_16x16x32_bf16 v[124:127], v[212:215], v[68:71], v[124:127]\n"
    "ds_read_b64_tr_b16 v[50:51], v15 offset:59392\n"
    "v_mfma_f32_16x16x32_bf16 v[112:115], v[216:219], v[72:75], v[112:115]\n"
    "ds_read_b64_tr_b16 v[52:53], v14 offset:61440\n"
    "v_mfma_f32_16x16x32_bf16 v[116:119], v[220:223], v[72:75], v[116:119]\n"
    "ds_read_b64_tr_b16 v[54:55], v14 offset:63488\n"
    "v_mfma_f32_16x16x32_bf16 v[120:123], v[216:219], v[76:79], v[120:123]\n"
    "ds_read_b64_tr_b16 v[56:57], v15 offset:61440\n"
    "v_mfma_f32_16x16x32_bf16 v[124:127], v[220:223], v[76:79], v[124:127]\n"
    "ds_read_b64_tr_b16 v[58:59], v15 offset:63488\n"
    "s_waitcnt lgkmcnt(8)\n"
    "v_mfma_f32_16x16x32_bf16 v[112:115], v[224:227], v[28:31], v[112:115]\n"
    "v_mfma_f32_16x16x32_bf16 v[116:119], v[228:231], v[28:31], v[116:119]\n"
    "v_mfma_f32_16x16x32_bf16 v[120:123], v[224:227], v[32:35], v[120:123]\n"
    "v_mfma_f32_16x16x32_bf16 v[124:127], v[228:231], v[32:35], v[124:127]\n"
    "v_mfma_f32_16x16x32_bf16 v[112:115], v[232:235], v[36:39], v[112:115]\n"
    "v_mfma_f32_16x16x32_bf16 v[116:119], v[236:239], v[36:39], v[116:119]\n"
    "v_mfma_f32_16x16x32_bf16 v[120:123], v[232:235], v[40:43], v[120:123]\n"
    "v_mfma_f32_16x16x32_bf16 v[124:127], v[236:239], v[40:43], v[124:127]\n"
    "s_waitcnt lgkmcnt(0)\n"
    "s_barrier\n"
    "v_mfma_f32_16x16x32_bf16 v[112:115], v[240:243], v[44:47], v[112:115]\n"
    "v_mfma_f32_16x16x32_bf16 v[116:119], v[244:247], v[44:47], v[116:119]\n"
    "v_mfma_f32_16x16x32_bf16 v[120:123], v[240:243], v[48:51], v[120:123]\n"
    "v_mfma_f32_16x16x32_bf16 v[124:127], v[244:247], v[48:51], v[124:127]\n"
    "v_mfma_f32_16x16x32_bf16 v[112:115], v[248:251], v[52:55], v[112:115]\n"
    "v_mfma_f32_16x16x32_bf16 v[116:119], v[252:255], v[52:55], v[116:119]\n"
    "v_mfma_f32_16x16x32_bf16 v[120:123], v[248:251], v[56:59], v[120:123]\n"
    "v_mfma_f32_16x16x32_bf16 v[124:127], v[252:255], v[56:59], v[124:127]\n"
    "s_nop 4\n"
    "v_fma_f32 v112, -v10, v20, v112\n"
    "v_fma_f32 v113, -v10, v21, v113\n"
    "v_fma_f32 v114, -v10, v22, v114\n"
    "v_fma_f32 v115, -v10, v23, v115\n"
    "v_fma_f32 v116, -v10, v24, v116\n"
    "v_fma_f32 v117, -v10, v25, v117\n"
    "v_fma_f32 v118, -v10, v26, v118\n"
    "v_fma_f32 v119, -v10, v27, v119\n"
    "v_cvt_pk_bf16_f32 v60, v112, v113\n"
    "v_cvt_pk_bf16_f32 v61, v114, v115\n"
    "v_cvt_pk_bf16_f32 v62, v116, v117\n"
    "v_cvt_pk_bf16_f32 v63, v118, v119\n"
    "v_pk_max_i16 v60, v60, 0\n"
    "v_pk_max_i16 v61, v61, 0\n"
    "v_pk_max_i16 v62, v62, 0\n"
    "v_pk_max_i16 v63, v63, 0\n"
    "buffer_store_dwordx4 v[60:63], v2, s[28:31], s56 offen offset:0 sc1\n"
    "v_fma_f32 v120, -v11, v20, v120\n"
    "v_fma_f32 v121, -v11, v21, v121\n"
    "v_fma_f32 v122, -v11, v22, v122\n"
    "v_fma_f32 v123, -v11, v23, v123\n"
    "v_fma_f32 v124, -v11, v24, v124\n"
    "v_fma_f32 v125, -v11, v25, v125\n"
    "v_fma_f32 v126, -v11, v26, v126\n"
    "v_fma_f32 v127, -v11, v27, v127\n"
    "v_cvt_pk_bf16_f32 v16, v120, v121\n"
    "v_cvt_pk_bf16_f32 v17, v122, v123\n"
    "v_cvt_pk_bf16_f32 v18, v124, v125\n"
    "v_cvt_pk_bf16_f32 v19, v126, v127\n"
    "v_pk_max_i16 v16, v16, 0\n"
    "v_pk_max_i16 v17, v17, 0\n"
    "v_pk_max_i16 v18, v18, 0\n"
    "v_pk_max_i16 v19, v19, 0\n"
    "buffer_store_dwordx4 v[16:19], v2, s[28:31], s56 offen offset:1024 sc1\n"
    "s_add_u32 s56, s56, 114688\n"
    "v_mov_b32 v6, s64\n"
    "v_mov_b32 v7, s65\n"
    "s_waitcnt vmcnt(0)\n"
    "s_mov_b64 exec, 1\n"
    "buffer_store_dwordx2 v[6:7], off, s[28:31], s59 sc1\n"
    "s_mov_b64 exec, -1\n"
    "s_add_u32 s59, s59, 114688\n"
    ".Lh3_A_post:\n"
    "s_setprio 0\n"
    "v_lshrrev_b32 v2, 2, v0\n"
    "v_and_b32 v3, 3, v0\n"
    "s_and_b32 s57, s11, 1\n"
    "s_lshl_b32 s57, s57, 2\n"
    "v_add_u32 v4, s57, v3\n"
    "v_bfe_u32 v5, v0, 3, 2\n"
    "v_lshlrev_b32 v5, 1, v5\n"
    "v_xor_b32 v4, v4, v5\n"
    "v_lshlrev_b32 v4, 4, v4\n"
    "v_lshl_or_b32 v4, v2, 7, v4\n"
    "s_lshr_b32 s56, s11, 1\n"
    "s_lshl_b32 s56, s56, 17\n"
    "s_lshl_b32 s57, s12, 13\n"
    "s_add_u32 s56, s56, s57\n"
    "s_add_u32 s56, s56, 4292608\n"
    "s_add_u32 s57, s56, 65536\n"
    "v_bfe_u32 v13, v0, 3, 4\n"
    "v_lshlrev_b32 v13, 4, v13\n"
    "v_lshrrev_b32 v14, 7, v0\n"
    "v_lshl_or_b32 v13, v14, 2, v13\n"
    "s_lshr_b32 s60, s11, 1\n"
    "s_lshl_b32 s60, s60, 9\n"
    "s_and_b32 s61, s11, 1\n"
    "s_lshl_b32 s61, s61, 3\n"
    "s_add_u32 s60, s60, s61\n"
    "s_add_u32 s60, s60, 40960\n"
    "buffer_load_dwordx4 v[16:19], v4, s[28:31], s56 offen sc1\n"
    "buffer_load_dwordx4 v[20:23], v4, s[28:31], s57 offen sc1\n"
    "buffer_load_dword v24, v13, s[28:31], s60 offen sc1\n"
    "buffer_load_dword v26, v13, s[28:31], s60 offen offset:256 sc1\n"
    "v_lshlrev_b32 v5, 4, v0\n"
    "v_add_u32 v5, 151552, v5\n"
    "v_and_b32 v2, 7, v0\n"
    "v_lshrrev_b32 v3, 3, v0\n"
    "v_lshlrev_b32 v10, 1, v3\n"
    "v_lshl_or_b32 v10, v2, 9, v10\n"
    "v_add_u32 v10, 151552, v10\n"
    "v_lshrrev_b32 v6, 2, v3\n"
    "v_and_b32 v7, 3, v3\n"
    "v_lshl_add_u32 v6, v6, 4, v7\n"
    "v_lshlrev_b32 v6, 10, v6\n"
    "v_lshl_or_b32 v6, v2, 4, v6\n"
    "s_lshr_b32 s58, s11, 2\n"
    "s_lshl_b32 s58, s58, 18\n"
    "s_and_b32 s59, s11, 2\n"
    "s_lshl_b32 s59, s59, 12\n"
    "s_add_u32 s58, s58, s59\n"
    "s_and_b32 s59, s11, 1\n"
    "s_lshl_b32 s59, s59, 17\n"
    "s_add_u32 s58, s58, s59\n"
    "s_lshl_b32 s59, s12, 7\n"
    "s_add_u32 s58, s58, s59\n"
    "s_add_u32 s59, s58, 4096\n"
    "v_and_b32 v7, 15, v3\n"
    "v_lshlrev_b32 v7, 8, v7\n"
    "v_lshrrev_b32 v8, 4, v3\n"
    "v_lshl_or_b32 v7, v8, 14, v7\n"
    "v_and_b32 v8, 7, v3\n"
    "v_lshlrev_b32 v9, 1, v2\n"
    "v_xor_b32 v8, v8, v9\n"
    "v_lshl_or_b32 v7, v8, 4, v7\n"
    "v_xor_b32 v8, 16, v7\n"
    "v_add_u32 v9, 98304, v7\n"
    "v_add_u32 v11, 98304, v8\n"
    "s_waitcnt vmcnt(0)\n"
    "v_max3_u32 v12, v16, v17, v18\n"
    "v_max3_u32 v12, v12, v19, v20\n"
    "v_max3_u32 v12, v12, v21, v22\n"
    "v_max3_u32 v12, v12, v23, v24\n"
    "v_max_u32 v12, v12, v26\n"
    "v_cmp_eq_u32 vcc, -1, v12\n"
    "s_cbranch_vccnz .Lh3_slow_10\n"
    ".Lh3_back_11:\n"
    "ds_write_b128 v5, v[16:19]\n"
    "ds_write_b128 v5, v[20:23] offset:4096\n"
    "s_waitcnt lgkmcnt(0)\n"
    "s_barrier\n"
    "ds_read_u16 v32, v10 offset:0\n"
    "ds_read_u16 v33, v10 offset:64\n"
    "ds_read_u16 v34, v10 offset:128\n"
    "ds_read_u16 v35, v10 offset:192\n"
    "ds_read_u16 v36, v10 offset:256\n"
    "ds_read_u16 v37, v10 offset:320\n"
    "ds_read_u16 v38, v10 offset:384\n"
    "ds_read_u16 v39, v10 offset:448\n"
    "s_waitcnt lgkmcnt(0)\n"
    "ds_read_u16 v40, v10 offset:4096\n"
    "ds_read_u16 v41, v10 offset:4160\n"
    "ds_read_u16 v42, v10 offset:4224\n"
    "ds_read_u16 v43, v10 offset:4288\n"
    "ds_read_u16 v44, v10 offset:4352\n"
    "ds_read_u16 v45, v10 offset:4416\n"
    "ds_read_u16 v46, v10 offset:4480\n"
    "ds_read_u16 v47, v10 offset:4544\n"
    "s_waitcnt lgkmcnt(0)\n"
    "v_lshlrev_b32 v32, 16, v32\n"
    "v_lshlrev_b32 v33, 16, v33\n"
    "v_lshlrev_b32 v34, 16, v34\n"
    "v_lshlrev_b32 v35, 16, v35\n"
    "v_lshlrev_b32 v36, 16, v36\n"
    "v_lshlrev_b32 v37, 16, v37\n"
    "v_lshlrev_b32 v38, 16, v38\n"
    "v_lshlrev_b32 v39, 16, v39\n"
    "v_lshlrev_b32 v40, 16, v40\n"
    "v_lshlrev_b32 v41, 16, v41\n"
    "v_lshlrev_b32 v42, 16, v42\n"
    "v_lshlrev_b32 v43, 16, v43\n"
    "v_lshlrev_b32 v44, 16, v44\n"
    "v_lshlrev_b32 v45, 16, v45\n"
    "v_lshlrev_b32 v46, 16, v46\n"
    "v_lshlrev_b32 v47, 16, v47\n"
    "ds_read_b128 v[64:67], v9 offset:0\n"
    "ds_read_b128 v[68:71], v11 offset:0\n"
    "ds_read_b128 v[72:75], v9 offset:4096\n"
    "ds_read_b128 v[76:79], v11 offset:4096\n"
    "ds_read_b128 v[80:83], v9 offset:8192\n"
    "ds_read_b128 v[84:87], v11 offset:8192\n"
    "ds_read_b128 v[88:91], v9 offset:12288\n"
    "ds_read_b128 v[92:95], v11 offset:12288\n"
    "s_waitcnt lgkmcnt(4)\n"
    "v_add_f32 v64, v64, v72\n"
    "v_add_f32 v65, v65, v73\n"
    "v_add_f32 v66, v66, v74\n"
    "v_add_f32 v67, v67, v75\n"
    "v_add_f32 v68, v68, v76\n"
    "v_add_f32 v69, v69, v77\n"
    "v_add_f32 v70, v70, v78\n"
    "v_add_f32 v71, v71, v79\n"
    "s_waitcnt lgkmcnt(0)\n"
    "v_add_f32 v80, v80, v88\n"
    "v_add_f32 v81, v81, v89\n"
    "v_add_f32 v82, v82, v90\n"
    "v_add_f32 v83, v83, v91\n"
    "v_add_f32 v84, v84, v92\n"
    "v_add_f32 v85, v85, v93\n"
    "v_add_f32 v86, v86, v94\n"
    "v_add_f32 v87, v87, v95\n"
    "v_add_f32 v64, v64, v80\n"
    "v_add_f32 v65, v65, v81\n"
    "v_add_f32 v66, v66, v82\n"
    "v_add_f32 v67, v67, v83\n"
    "v_add_f32 v68, v68, v84\n"
    "v_add_f32 v69, v69, v85\n"
    "v_add_f32 v70, v70, v86\n"
    "v_add_f32 v71, v71, v87\n"
    "v_fma_f32 v64, v64, v24, v32\n"
    "v_fma_f32 v65, v65, v24, v33\n"
    "v_fma_f32 v66, v66, v24, v34\n"
    "v_fma_f32 v67, v67, v24, v35\n"
    "v_fma_f32 v68, v68, v24, v36\n"
    "v_fma_f32 v69, v69, v24, v37\n"
    "v_fma_f32 v70, v70, v24, v38\n"
    "v_fma_f32 v71, v71, v24, v39\n"
    "v_cvt_pk_bf16_f32 v12, v64, v65\n"
    "v_cvt_pk_bf16_f32 v13, v66, v67\n"
    "v_cvt_pk_bf16_f32 v14, v68, v69\n"
    "v_cvt_pk_bf16_f32 v15, v70, v71\n"
    "buffer_store_dwordx4 v[12:15], v6, s[52:55], s58 offen sc1\n"
    "s_barrier\n"
    "s_setprio 3\n"
    "ds_read_b128 v[64:67], v7 offset:0\n"
    "ds_read_b128 v[68:71], v8 offset:0\n"
    "ds_read_b128 v[72:75], v7 offset:4096\n"
    "ds_read_b128 v[76:79], v8 offset:4096\n"
    "ds_read_b128 v[80:83], v7 offset:8192\n"
    "ds_read_b128 v[84:87], v8 offset:8192\n"
    "ds_read_b128 v[88:91], v7 offset:12288\n"
    "ds_read_b128 v[92:95], v8 offset:12288\n"
    "s_waitcnt lgkmcnt(4)\n"
    "v_pk_add_f32 v[64:65], v[64:65], v[72:73]\n"
    "v_pk_add_f32 v[66:67], v[66:67], v[74:75]\n"
    "v_pk_add_f32 v[68:69], v[68:69], v[76:77]\n"
    "v_pk_add_f32 v[70:71], v[70:71], v[78:79]\n"
    "s_waitcnt lgkmcnt(0)\n"
    "v_pk_add_f32 v[80:81], v[80:81], v[88:89]\n"
    "v_pk_add_f32 v[82:83], v[82:83], v[90:91]\n"
    "v_pk_add_f32 v[84:85], v[84:85], v[92:93]\n"
    "v_pk_add_f32 v[86:87], v[86:87], v[94:95]\n"
    "v_pk_add_f32 v[64:65], v[64:65], v[80:81]\n"
    "v_pk_add_f32 v[66:67], v[66:67], v[82:83]\n"
    "v_pk_add_f32 v[68:69], v[68:69], v[84:85]\n"
    "v_pk_add_f32 v[70:71], v[70:71], v[86:87]\n"
    "v_pk_fma_f32 v[64:65], v[64:65], v[26:27], v[40:41] op_sel_hi:[1,0,1]\n"
    "v_pk_fma_f32 v[66:67], v[66:67], v[26:27], v[42:43] op_sel_hi:[1,0,1]\n"
    "v_pk_fma_f32 v[68:69], v[68:69], v[26:27], v[44:45] op_sel_hi:[1,0,1]\n"
    "v_pk_fma_f32 v[70:71], v[70:71], v[26:27], v[46:47] op_sel_hi:[1,0,1]\n"
    "v_cvt_pk_bf16_f32 v12, v64, v65\n"
    "v_cvt_pk_bf16_f32 v13, v66, v67\n"
    "v_cvt_pk_bf16_f32 v14, v68, v69\n"
    "v_cvt_pk_bf16_f32 v15, v70, v71\n"
    "buffer_store_dwordx4 v[12:15], v6, s[52:55], s59 offen sc1\n"
    "s_endpgm\n"
    ".Lh3_A_spare:\n"
    "v_lshrrev_b32 v16, 2, v1\n"
    "v_lshlrev_b32 v16, 7, v16\n"
    "v_and_b32 v17, 3, v1\n"
    "v_lshl_or_b32 v16, v17, 3, v16\n"
    "v_bfe_u32 v17, v1, 3, 2\n"
    "v_lshl_or_b32 v8, v17, 5, v16\n"
    "v_xor_b32 v9, 32, v8\n"
    "v_xor_b32 v10, 64, v8\n"
    "v_xor_b32 v11, 96, v8\n"
    "s_lshl_b32 s63, s13, 12\n"
    "s_add_u32 s63, s63, 32768\n"
    "v_lshlrev_b32 v5, 4, v1\n"
    "v_mov_b32 v19, 0x3f803f80\n"
    "v_add_u32 v8, s63, v8\n"
    "v_add_u32 v9, s63, v9\n"
    "v_add_u32 v10, s63, v10\n"
    "v_add_u32 v11, s63, v11\n"
    "s_add_u32 s66, s13, 4\n"
    "s_branch .Lh3_SP_stats\n"
    ".Lh3_A_spare2:\n"
    "s_barrier\n"
    "s_branch .Lh3_A_post\n"
    ".Lh3_TEAMB:\n"
    "s_lshl_b32 s8, s4, 5\n"
    "s_add_u32 s8, s8, s5\n"
    "s_and_b32 s56, s5, 3\n"
    "s_lshr_b32 s9, s4, 1\n"
    "s_lshl2_add_u32 s9, s9, s56\n"
    "s_lshl2_add_u32 s11, s4, s56\n"
    "s_lshr_b32 s12, s5, 2\n"
    "s_and_b32 s10, s4, 1\n"
    "s_mul_i32 s10, s10, 7\n"
    "s_add_u32 s10, s10, s12\n"
    "s_movk_i32 s14, 0x4000\n"
    "s_mov_b32 s3, 0x100000\n"
    "s_mov_b32 s15, 0xffff0000\n"
    "v_lshlrev_b32 v5, 4, v1\n"
    "s_bitcmp1_b32 s13, 0\n"
    "s_cbranch_scc1 .Lh3_B_odd\n"
    "v_mov_b32 v8, 0\n"
    "v_mov_b32 v9, 0\n"
    "v_mov_b32 v10, 0\n"
    "v_mov_b32 v11, 0\n"
    "s_cmp_eq_u32 s13, 0\n"
    "s_mov_b32 s58, 157072\n"
    "s_cselect_b32 s58, 114688, s58\n"
    "s_mov_b32 s56, 121856\n"
    "s_cselect_b32 s56, 118848, s56\n"
    "s_cselect_b32 s57, 0, 4096\n"
    "v_lshrrev_b32 v4, 5, v1\n"
    "v_mul_u32_u24 v4, s57, v4\n"
    "v_add3_u32 v4, v4, v5, s56\n"
    "ds_write_b128 v4, v[8:11]\n"
    "v_bfe_u32 v4, v1, 1, 1\n"
    "v_mul_u32_u24 v4, 4128, v4\n"
    "v_and_b32 v6, 16, v5\n"
    "v_add3_u32 v4, v4, v6, s58\n"
    "ds_write_b128 v4, v[8:11]\n"
    "s_lshl_b32 s56, s8, 13\n"
    "s_lshl_b32 s57, s13, 11\n"
    "s_add_u32 s56, s56, s57\n"
    "s_add_u32 m0, s58, 32\n"
    "s_lshl_b32 s59, s8, 2\n"
    "s_waitcnt lgkmcnt(0)\n"
    "s_barrier\n"
    "buffer_load_dwordx4 v5, s[36:39], s56 offen offset:0 nt lds\n"
    "buffer_load_dwordx4 v5, s[36:39], s56 offen offset:1024 nt lds\n"
    "buffer_load_dwordx4 v5, s[36:39], s56 offen offset:2048 nt lds\n"
    "buffer_load_dwordx4 v5, s[36:39], s56 offen offset:3072 nt lds\n"
    "buffer_load_dword v26, off, s[40:43], s59\n"
    "s_branch .Lh3_B_go\n"
    ".Lh3_B_odd:\n"
    "s_barrier\n"
    "s_waitcnt lgkmcnt(0)\n"
    ".Lh3_B_go:\n"
    "buffer_load_dwordx2 v[24:25], off, s[28:31], 0 sc1\n"
    "v_lshrrev_b32 v2, 4, v1\n"
    "v_and_b32 v3, 15, v1\n"
    "v_lshlrev_b32 v14, 1, v2\n"
    "v_xor_b32 v14, v14, v3\n"
    "v_lshlrev_b32 v14, 4, v14\n"
    "v_mul_u32_u24 v2, 3584, v2\n"
    "v_xor_b32 v17, 0x80, v14\n"
    "v_add_u32 v14, v14, v2\n"
    "v_add_u32 v17, v17, v2\n"
    "v_lshrrev_b32 v2, 4, v1\n"
    "v_lshlrev_b32 v2, 1, v2\n"
    "v_and_b32 v3, 7, v1\n"
    "v_xor_b32 v2, v2, v3\n"
    "v_lshlrev_b32 v2, 4, v2\n"
    "v_lshrrev_b32 v4, 3, v1\n"
    "v_lshl_or_b32 v13, v4, 10, v2\n"
    "s_and_b32 s56, s8, 3\n"
    "s_lshl_b32 s56, s56, 1\n"
    "v_xor_b32 v2, s56, v3\n"
    "v_lshlrev_b32 v2, 4, v2\n"
    "v_lshl_or_b32 v12, v4, 16, v2\n"
    "v_mov_b32 v15, 118848\n"
    "v_mov_b32 v16, 1\n"
    "v_lshrrev_b32 v2, 5, v1\n"
    "v_bfe_u32 v6, v1, 3, 2\n"
    "v_lshl_add_u32 v6, v2, 4, v6\n"
    "v_lshlrev_b32 v6, 10, v6\n"
    "v_lshl_or_b32 v6, v3, 4, v6\n"
    "v_bfe_u32 v2, v0, 3, 5\n"
    "v_lshlrev_b32 v7, 4, v3\n"
    "v_lshl_or_b32 v7, v2, 13, v7\n"
    "v_mul_u32_u24 v18, 1040, v3\n"
    "v_lshl_add_u32 v18, v2, 2, v18\n"
    "v_add_u32 v18, 148752, v18\n"
    "v_mov_b32 v20, -1\n"
    "v_mov_b32 v21, -1\n"
    "v_mov_b32 v22, -1\n"
    "v_mov_b32 v23, -1\n"
    "s_cmp_ge_u32 s5, 28\n"
    "s_cselect_b32 s50, 0, s50\n"
    "s_lshl_b32 s56, s4, 18\n"
    "s_and_b32 s57, s6, 3\n"
    "s_lshl_b32 s57, s57, 12\n"
    "s_add_u32 s57, s56, s57\n"
    "s_lshl_b32 s59, s13, 15\n"
    "s_add_u32 s57, s57, s59\n"
    "s_lshl_b32 s59, s7, 7\n"
    "s_add_u32 s57, s57, s59\n"
    "s_mul_i32 s59, s13, 1040\n"
    "s_add_u32 m0, s59, 148752\n"
    "s_add_u32 s60, s57, 131072\n"
    "buffer_load_dwordx4 v6, s[32:35], s57 offen  lds\n"
    "s_add_u32 m0, s59, 152912\n"
    "s_lshr_b32 s61, s5, 2\n"
    "buffer_load_dwordx4 v6, s[32:35], s60 offen  lds\n"
    "s_lshl_b32 s57, s13, 16\n"
    "s_add_u32 s56, s56, s57\n"
    "s_lshr_b32 s57, s61, 2\n"
    "s_lshl_b32 s57, s57, 14\n"
    "s_add_u32 s56, s56, s57\n"
    "s_and_b32 s57, s61, 3\n"
    "s_lshl_b32 s57, s57, 10\n"
    "s_add_u32 s56, s56, s57\n"
    "s_and_b32 s57, s5, 3\n"
    "s_lshl_b32 s57, s57, 12\n"
    "s_add_u32 s56, s56, s57\n"
    "s_add_u32 s57, s56, 32768\n"
    "buffer_load_dwordx4 v[28:31], v5, s[32:35], s56 offen\n"
    "buffer_load_dwordx4 v[32:35], v5, s[32:35], s57 offen\n"
    "s_lshl_b32 s58, s7, 18\n"
    "s_lshl_b32 s59, s6, 7\n"
    "s_add_u32 s58, s58, s59\n"
    "s_add_u32 s58, s58, 2162688\n"
    "s_add_u32 s59, s58, 4096\n"
    "s_lshl_b32 s60, s13, 19\n"
    "s_lshl_b32 s61, s8, 8\n"
    "s_add_u32 s60, s60, s61\n"
    "s_add_u32 s60, s60, 4292608\n"
    "buffer_store_dwordx4 v[20:23], v12, s[28:31], s60 offen sc1\n"
    "buffer_store_dwordx4 v[20:23], v12, s[28:31], s60 offen offset:128 sc1\n"
    "s_cmp_ge_u32 s5, 28\n"
    "s_cselect_b32 s67, 0, 2147483648\n"
    "v_and_b32 v3, 0xf0, v5\n"
    "v_bfe_u32 v2, v1, 4, 1\n"
    "v_lshl_or_b32 v3, v2, 13, v3\n"
    "v_or_b32 v3, s67, v3\n"
    "s_and_b32 s66, s4, 1\n"
    "s_lshl1_add_u32 s66, s9, s66\n"
    "s_lshl_b32 s66, s66, 8\n"
    "s_add_u32 s66, s66, 32768\n"
    "buffer_store_dwordx4 v[20:23], v3, s[28:31], s66 offen sc1\n"
    "s_mul_i32 s16, s12, 0\n"
    "s_and_b32 s17, s4, 7\n"
    "s_mul_i32 s17, s17, 7\n"
    "s_lshr_b32 s16, s16, 2\n"
    "s_lshr_b32 s17, s17, 2\n"
    "s_add_u32 s16, s16, s17\n"
    "s_sub_u32 s17, s16, 14\n"
    "s_cmp_ge_u32 s16, 14\n"
    "s_cselect_b32 s16, s17, s16\n"
    "s_lshl_b32 s16, s16, 11\n"
    "s_add_u32 s17, s16, 2048\n"
    "s_cmp_eq_u32 s17, 28672\n"
    "s_cselect_b32 s17, 0, s17\n"
    "s_add_u32 s18, s17, 2048\n"
    "s_cmp_eq_u32 s18, 28672\n"
    "s_cselect_b32 s18, 0, s18\n"
    "s_add_u32 s19, s18, 2048\n"
    "s_cmp_eq_u32 s19, 28672\n"
    "s_cselect_b32 s19, 0, s19\n"
    "s_add_u32 s20, s19, 2048\n"
    "s_cmp_eq_u32 s20, 28672\n"
    "s_cselect_b32 s20, 0, s20\n"
    "s_add_u32 s21, s20, 2048\n"
    "s_cmp_eq_u32 s21, 28672\n"
    "s_cselect_b32 s21, 0, s21\n"
    "s_add_u32 s22, s21, 2048\n"
    "s_cmp_eq_u32 s22, 28672\n"
    "s_cselect_b32 s22, 0, s22\n"
    "s_add_u32 s23, s22, 2048\n"
    "s_cmp_eq_u32 s23, 28672\n"
    "s_cselect_b32 s23, 0, s23\n"
    "s_add_u32 s24, s23, 2048\n"
    "s_cmp_eq_u32 s24, 28672\n"
    "s_cselect_b32 s24, 0, s24\n"
    "s_add_u32 s25, s24, 2048\n"
    "s_cmp_eq_u32 s25, 28672\n"
    "s_cselect_b32 s25, 0, s25\n"
    "s_add_u32 s26, s25, 2048\n"
    "s_cmp_eq_u32 s26, 28672\n"
    "s_cselect_b32 s26, 0, s26\n"
    "s_add_u32 s27, s26, 2048\n"
    "s_cmp_eq_u32 s27, 28672\n"
    "s_cselect_b32 s27, 0, s27\n"
    "s_add_u32 s52, s27, 2048\n"
    "s_cmp_eq_u32 s52, 28672\n"
    "s_cselect_b32 s52, 0, s52\n"
    "s_add_u32 s53, s52, 2048\n"
    "s_cmp_eq_u32 s53, 28672\n"
    "s_cselect_b32 s53, 0, s53\n"
    "s_lshr_b32 s38, s5, 2\n"
    "s_mul_i32 s38, s38, 0\n"
    "s_lshr_b32 s38, s38, 4\n"
    "s_lshr_b32 s57, s4, 1\n"
    "s_add_u32 s38, s38, s57\n"
    "s_and_b32 s38, s38, 15\n"
    "s_mul_i32 s54, s13, 458752\n"
    "s_lshl_b32 s57, s12, 7\n"
    "s_add_u32 s54, s54, s57\n"
    "s_add_u32 s55, s54, 458752\n"
    "s_lshl4_add_u32 s62, s16, s54\n"
    "s_mul_i32 s63, s13, 28672\n"
    "s_lshl_b32 s36, s10, 8\n"
    "s_add_u32 s37, s36, 1835008\n"
    "s_mul_i32 s57, s13, 28\n"
    "s_lshl3_add_u32 s38, s38, s57\n"
    "s_and_b32 s38, s38, 127\n"
    "s_mul_i32 s38, s38, 14336\n"
    "s_add_u32 s38, s38, s36\n"
    "v_lshrrev_b32 v2, 2, v1\n"
    "v_lshlrev_b32 v2, 7, v2\n"
    "v_and_b32 v3, 3, v1\n"
    "v_lshl_or_b32 v2, v3, 3, v2\n"
    "v_bfe_u32 v3, v1, 3, 2\n"
    "v_lshl_or_b32 v8, v3, 5, v2\n"
    "v_xor_b32 v9, 32, v8\n"
    "v_xor_b32 v10, 64, v8\n"
    "v_xor_b32 v11, 96, v8\n"
    "v_lshrrev_b32 v2, 5, v1\n"
    "v_and_b32 v3, 31, v1\n"
    "v_lshlrev_b32 v3, 4, v3\n"
    "v_lshl_or_b32 v4, v2, 12, v3\n"
    "s_lshl_b32 s40, s13, 13\n"
    "s_add_u32 s40, s40, 4259840\n"
    "s_lshl_b32 s41, s8, 13\n"
    "s_lshl_b32 s42, s13, 10\n"
    "s_add_u32 s41, s41, s42\n"
    "s_add_u32 s41, s41, 2162688\n"
    "s_add_u32 s42, s41, 4096\n"
    "v_add_u32 v112, s63, v8\n"
    "v_add_u32 v113, s63, v9\n"
    "v_add_u32 v114, s63, v10\n"
    "v_add_u32 v115, s63, v11\n"
    "s_add_u32 m0, s63, 0\n"
    "s_mov_b32 s67, 3\n"
    ".Lh3_dma_12:\n"
    "s_waitcnt vmcnt(8)\n"
    "buffer_load_dwordx4 v13, s[44:47], s62 offen lds\n"
    "s_addk_i32 m0, 0x400\n"
    "s_addk_i32 s62, 8192\n"
    "buffer_load_dwordx4 v13, s[44:47], s62 offen lds\n"
    "s_addk_i32 m0, 0x400\n"
    "s_addk_i32 s62, 8192\n"
    "s_cmp_eq_u32 s62, s55\n"
    "s_cselect_b32 s62, s54, s62\n"
    "s_sub_u32 s67, s67, 1\n"
    "s_cmp_lg_u32 s67, 0\n"
    "s_cbranch_scc1 .Lh3_dma_12\n"
    "s_waitcnt vmcnt(14)\n"
    "s_bitcmp1_b32 s13, 0\n"
    "s_cbranch_scc1 .Lh3_B_nofix\n"
    "s_cmp_eq_u32 s13, 0\n"
    "s_mov_b32 s56, 157104\n"
    "s_cselect_b32 s56, 114720, s56\n"
    "s_cselect_b32 s57, 16, 0\n"
    "v_mov_b32 v2, s56\n"
    "ds_read_u16 v3, v2\n"
    "v_lshlrev_b32 v26, s57, v26\n"
    "v_and_b32 v26, s15, v26\n"
    "s_waitcnt lgkmcnt(0)\n"
    "v_lshlrev_b32 v3, 16, v3\n"
    "v_add_f32 v3, v3, v26\n"
    "v_cvt_pk_bf16_f32 v3, v3, v3\n"
    "ds_write_b16 v2, v3\n"
    "s_mov_b64 exec, 1\n"
    "ds_add_u32 v15, v16 offset:4\n"
    "s_mov_b64 exec, -1\n"
    ".Lh3_B_nofix:\n"
    "s_waitcnt vmcnt(13)\n"
    "v_readfirstlane_b32 s64, v24\n"
    "v_readfirstlane_b32 s65, v25\n"
    "s_mul_i32 s64, s64, 1664525\n"
    "s_mul_i32 s65, s65, 22695477\n"
    "s_add_u32 s64, s64, 1013904223\n"
    "s_add_u32 s65, s65, 0x3c6ef35f\n"
    "v_mov_b32 v24, s64\n"
    "v_mov_b32 v25, s65\n"
    "s_waitcnt vmcnt(11)\n"
    "s_mov_b64 exec, 1\n"
    "ds_add_u32 v15, v16 offset:20\n"
    "s_mov_b64 exec, -1\n"
    "s_waitcnt vmcnt(9)\n"
    "v_mov_b32 v19, 0x3f803f80\n"
    "v_dot2_f32_bf16 v36, v28, v19, 0\n"
    "v_dot2_f32_bf16 v37, v28, v28, 0\n"
    "v_dot2_f32_bf16 v38, v32, v19, 0\n"
    "v_dot2_f32_bf16 v39, v32, v32, 0\n"
    "v_dot2_f32_bf16 v36, v29, v19, v36\n"
    "v_dot2_f32_bf16 v37, v29, v29, v37\n"
    "v_dot2_f32_bf16 v38, v33, v19, v38\n"
    "v_dot2_f32_bf16 v39, v33, v33, v39\n"
    "v_dot2_f32_bf16 v36, v30, v19, v36\n"
    "v_dot2_f32_bf16 v37, v30, v30, v37\n"
    "v_dot2_f32_bf16 v38, v34, v19, v38\n"
    "v_dot2_f32_bf16 v39, v34, v34, v39\n"
    "v_dot2_f32_bf16 v36, v31, v19, v36\n"
    "v_dot2_f32_bf16 v37, v31, v31, v37\n"
    "v_dot2_f32_bf16 v38, v35, v19, v38\n"
    "v_dot2_f32_bf16 v39, v35, v35, v39\n"
    "v_add_f32_dpp v36, v36, v36 row_ror:8 row_mask:0xf bank_mask:0xf\n"
    "v_add_f32_dpp v37, v37, v37 row_ror:8 row_mask:0xf bank_mask:0xf\n"
    "v_add_f32_dpp v38, v38, v38 row_ror:8 row_mask:0xf bank_mask:0xf\n"
    "v_add_f32_dpp v39, v39, v39 row_ror:8 row_mask:0xf bank_mask:0xf\n"
    "v_add_f32_dpp v36, v36, v36 row_ror:4 row_mask:0xf bank_mask:0xf\n"
    "v_add_f32_dpp v37, v37, v37 row_ror:4 row_mask:0xf bank_mask:0xf\n"
    "v_add_f32_dpp v38, v38, v38 row_ror:4 row_mask:0xf bank_mask:0xf\n"
    "v_add_f32_dpp v39, v39, v39 row_ror:4 row_mask:0xf bank_mask:0xf\n"
    "v_add_f32_dpp v36, v36, v36 row_ror:2 row_mask:0xf bank_mask:0xf\n"
    "v_add_f32_dpp v37, v37, v37 row_ror:2 row_mask:0xf bank_mask:0xf\n"
    "v_add_f32_dpp v38, v38, v38 row_ror:2 row_mask:0xf bank_mask:0xf\n"
    "v_add_f32_dpp v39, v39, v39 row_ror:2 row_mask:0xf bank_mask:0xf\n"
    "v_add_f32_dpp v36, v36, v36 row_ror:1 row_mask:0xf bank_mask:0xf\n"
    "v_add_f32_dpp v37, v37, v37 row_ror:1 row_mask:0xf bank_mask:0xf\n"
    "v_add_f32_dpp v38, v38, v38 row_ror:1 row_mask:0xf bank_mask:0xf\n"
    "v_add_f32_dpp v39, v39, v39 row_ror:1 row_mask:0xf bank_mask:0xf\n"
    "v_mov_b32 v40, v36\n"
    "v_mov_b32 v41, v37\n"
    "v_mov_b32 v42, v38\n"
    "v_mov_b32 v43, v39\n"
    "v_permlane16_swap_b32 v36, v40\n"
    "v_permlane16_swap_b32 v37, v41\n"
    "v_permlane16_swap_b32 v38, v42\n"
    "v_permlane16_swap_b32 v39, v43\n"
    "v_add_f32 v36, v36, v40\n"
    "v_add_f32 v37, v37, v41\n"
    "v_add_f32 v38, v38, v42\n"
    "v_add_f32 v39, v39, v43\n"
    "v_mov_b32 v40, v36\n"
    "v_mov_b32 v41, v37\n"
    "v_mov_b32 v42, v38\n"
    "v_mov_b32 v43, v39\n"
    "v_permlane32_swap_b32 v36, v40\n"
    "v_permlane32_swap_b32 v37, v41\n"
    "v_permlane32_swap_b32 v38, v42\n"
    "v_permlane32_swap_b32 v39, v43\n"
    "v_add_f32 v36, v36, v40\n"
    "v_add_f32 v37, v37, v41\n"
    "v_add_f32 v38, v38, v42\n"
    "v_add_f32 v39, v39, v43\n"
    "v_mul_f32 v40, 0x3b000000, v36\n"
    "v_mul_f32 v42, 0x3b000000, v37\n"
    "v_fma_f32 v42, -v40, v40, v42\n"
    "v_max_f32 v42, 0, v42\n"
    "v_add_f32 v42, 0x3727c5ac, v42\n"
    "v_rsq_f32 v42, v42\n"
    "v_mov_b32 v41, s64\n"
    "v_mov_b32 v43, s64\n"
    "v_mul_f32 v44, 0x3b000000, v38\n"
    "v_mul_f32 v46, 0x3b000000, v39\n"
    "v_fma_f32 v46, -v44, v44, v46\n"
    "v_max_f32 v46, 0, v46\n"
    "v_add_f32 v46, 0x3727c5ac, v46\n"
    "v_rsq_f32 v46, v46\n"
    "v_mov_b32 v45, s64\n"
    "v_mov_b32 v47, s64\n"
    "s_lshl_b32 s56, s13, 5\n"
    "s_add_u32 s56, s56, 118912\n"
    "v_mov_b32 v2, s56\n"
    "ds_write_b128 v2, v[40:43]\n"
    "ds_write_b128 v2, v[44:47] offset:16\n"
    ".Lh3_w_13:\n"
    "ds_read_b32 v2, v15 offset:20\n"
    "s_cmp_eq_u32 s3, 0\n"
    "s_cbranch_scc1 .Lh3_w_13x\n"
    "s_sub_u32 s3, s3, 1\n"
    "s_waitcnt lgkmcnt(0)\n"
    "v_cmp_gt_u32 vcc, 4, v2\n"
    "s_cbranch_vccnz .Lh3_w_13\n"
    ".Lh3_w_13x:\n"
    "ds_read2_b32 v[48:49], v18 offset0:0 offset1:32\n"
    "ds_read2_b32 v[50:51], v18 offset0:64 offset1:96\n"
    "ds_read2_b32 v[52:53], v18 offset0:128 offset1:160\n"
    "ds_read2_b32 v[54:55], v18 offset0:192 offset1:224\n"
    "s_mov_b32 s56, 0x05040100\n"
    "s_mov_b32 s57, 0x07060302\n"
    "s_waitcnt lgkmcnt(0)\n"
    "v_perm_b32 v28, v49, v48, s56\n"
    "v_perm_b32 v32, v49, v48, s57\n"
    "v_perm_b32 v29, v51, v50, s56\n"
    "v_perm_b32 v33, v51, v50, s57\n"
    "v_perm_b32 v30, v53, v52, s56\n"
    "v_perm_b32 v34, v53, v52, s57\n"
    "v_perm_b32 v31, v55, v54, s56\n"
    "v_perm_b32 v35, v55, v54, s57\n"
    "buffer_store_dwordx4 v[28:31], v7, s[28:31], s58 offen sc1\n"
    "buffer_store_dwordx4 v[32:35], v7, s[28:31], s59 offen sc1\n"
    "s_add_u32 m0, s63, 6144\n"
    "s_mov_b32 s67, 4\n"
    ".Lh3_dma_14:\n"
    "s_waitcnt vmcnt(8)\n"
    "buffer_load_dwordx4 v13, s[44:47], s62 offen lds\n"
    "s_addk_i32 m0, 0x400\n"
    "s_addk_i32 s62, 8192\n"
    "buffer_load_dwordx4 v13, s[44:47], s62 offen lds\n"
    "s_addk_i32 m0, 0x400\n"
    "s_addk_i32 s62, 8192\n"
    "s_cmp_eq_u32 s62, s55\n"
    "s_cselect_b32 s62, s54, s62\n"
    "s_sub_u32 s67, s67, 1\n"
    "s_cmp_lg_u32 s67, 0\n"
    "s_cbranch_scc1 .Lh3_dma_14\n"
    "s_waitcnt vmcnt(8)\n"
    "s_mov_b64 exec, 1\n"
    "ds_add_u32 v15, v16 offset:12\n"
    "s_mov_b64 exec, -1\n"
    "s_cmp_lg_u32 s13, 1\n"
    "s_cbranch_scc1 .Lh3_B_noS\n"
    ".Lh3_w_15:\n"
    "ds_read_b32 v2, v15 offset:12\n"
    "s_cmp_eq_u32 s3, 0\n"
    "s_cbranch_scc1 .Lh3_w_15x\n"
    "s_sub_u32 s3, s3, 1\n"
    "s_waitcnt lgkmcnt(0)\n"
    "v_cmp_gt_u32 vcc, 4, v2\n"
    "s_cbranch_vccnz .Lh3_w_15\n"
    ".Lh3_w_15x:\n"
    "v_add_u32 v2, 118912, v5\n"
    "s_lshl_b32 s56, s8, 7\n"
    "s_add_u32 s56, s56, 4259840\n"
    "s_mov_b64 exec, 0xff\n"
    "ds_read_b128 v[40:43], v2\n"
    "s_waitcnt lgkmcnt(0)\n"
    "buffer_store_dwordx4 v[40:43], v5, s[28:31], s56 offen sc1\n"
    "s_mov_b64 exec, -1\n"
    ".Lh3_B_noS:\n"
    ".Lh3_w_16:\n"
    "s_sleep 1\n"
    "ds_read_b32 v2, v15 offset:8\n"
    "s_cmp_eq_u32 s3, 0\n"
    "s_cbranch_scc1 .Lh3_w_16x\n"
    "s_sub_u32 s3, s3, 1\n"
    "s_waitcnt lgkmcnt(0)\n"
    "v_cmp_gt_u32 vcc, 4, v2\n"
    "s_cbranch_vccnz .Lh3_w_16\n"
    ".Lh3_w_16x:\n"
    "s_add_u32 m0, s63, 14336\n"
    "s_mov_b32 s67, 7\n"
    ".Lh3_dma_17:\n"
    "s_waitcnt vmcnt(8)\n"
    "buffer_load_dwordx4 v13, s[44:47], s62 offen lds\n"
    "s_addk_i32 m0, 0x400\n"
    "s_addk_i32 s62, 8192\n"
    "buffer_load_dwordx4 v13, s[44:47], s62 offen lds\n"
    "s_addk_i32 m0, 0x400\n"
    "s_addk_i32 s62, 8192\n"
    "s_cmp_eq_u32 s62, s55\n"
    "s_cselect_b32 s62, s54, s62\n"
    "s_sub_u32 s67, s67, 1\n"
    "s_cmp_lg_u32 s67, 0\n"
    "s_cbranch_scc1 .Lh3_dma_17\n"
    "buffer_load_dwordx4 v[48:51], v4, s[28:31], s40 offen offset:0 sc1\n"
    "buffer_load_dwordx4 v[52:55], v4, s[28:31], s40 offen offset:512 sc1\n"
    "buffer_load_dwordx4 v[56:59], v4, s[28:31], s40 offen offset:1024 sc1\n"
    "buffer_load_dwordx4 v[60:63], v4, s[28:31], s40 offen offset:1536 sc1\n"
    "buffer_load_dwordx4 v[64:67], v4, s[28:31], s40 offen offset:2048 sc1\n"
    "buffer_load_dwordx4 v[68:71], v4, s[28:31], s40 offen offset:2560 sc1\n"
    "buffer_load_dwordx4 v[72:75], v4, s[28:31], s40 offen offset:3072 sc1\n"
    "buffer_load_dwordx4 v[76:79], v4, s[28:31], s40 offen offset:3584 sc1\n"
    "s_waitcnt vmcnt(22)\n"
    "ds_read_b64_tr_b16 v[144:145], v112 offset:0\n"
    "ds_read_b64_tr_b16 v[148:149], v113 offset:0\n"
    "ds_read_b64_tr_b16 v[152:153], v114 offset:0\n"
    "ds_read_b64_tr_b16 v[156:157], v115 offset:0\n"
    "ds_read_b64_tr_b16 v[146:147], v112 offset:2048\n"
    "ds_read_b64_tr_b16 v[150:151], v113 offset:2048\n"
    "ds_read_b64_tr_b16 v[154:155], v114 offset:2048\n"
    "ds_read_b64_tr_b16 v[158:159], v115 offset:2048\n"
    "ds_read_b64_tr_b16 v[160:161], v112 offset:4096\n"
    "ds_read_b64_tr_b16 v[164:165], v113 offset:4096\n"
    "ds_read_b64_tr_b16 v[168:169], v114 offset:4096\n"
    "ds_read_b64_tr_b16 v[172:173], v115 offset:4096\n"
    "ds_read_b64_tr_b16 v[162:163], v112 offset:6144\n"
    "ds_read_b64_tr_b16 v[166:167], v113 offset:6144\n"
    "ds_read_b64_tr_b16 v[170:171], v114 offset:6144\n"
    "ds_read_b64_tr_b16 v[174:175], v115 offset:6144\n"
    "ds_read_b64_tr_b16 v[176:177], v112 offset:8192\n"
    "ds_read_b64_tr_b16 v[180:181], v113 offset:8192\n"
    "ds_read_b64_tr_b16 v[184:185], v114 offset:8192\n"
    "ds_read_b64_tr_b16 v[188:189], v115 offset:8192\n"
    "ds_read_b64_tr_b16 v[178:179], v112 offset:10240\n"
    "ds_read_b64_tr_b16 v[182:183], v113 offset:10240\n"
    "ds_read_b64_tr_b16 v[186:187], v114 offset:10240\n"
    "ds_read_b64_tr_b16 v[190:191], v115 offset:10240\n"
    "ds_read_b64_tr_b16 v[192:193], v112 offset:12288\n"
    "ds_read_b64_tr_b16 v[196:197], v113 offset:12288\n"
    "ds_read_b64_tr_b16 v[200:201], v114 offset:12288\n"
    "ds_read_b64_tr_b16 v[204:205], v115 offset:12288\n"
    "s_waitcnt lgkmcnt(0)\n"
    "s_add_u32 m0, s63, 0\n"
    "s_mov_b32 s67, 7\n"
    ".Lh3_dma_18:\n"
    "s_waitcnt vmcnt(8)\n"
    "buffer_load_dwordx4 v14, s[48:51], s38 offen lds\n"
    "s_addk_i32 m0, 0x400\n"
    "s_addk_i32 s38, 14336\n"
    "buffer_load_dwordx4 v17, s[48:51], s38 offen lds\n"
    "s_addk_i32 m0, 0x400\n"
    "s_addk_i32 s38, 14336\n"
    "s_cmp_eq_u32 s38, s37\n"
    "s_cselect_b32 s38, s36, s38\n"
    "s_sub_u32 s67, s67, 1\n"
    "s_cmp_lg_u32 s67, 0\n"
    "s_cbranch_scc1 .Lh3_dma_18\n"
    "s_waitcnt vmcnt(14)\n"
    "v_xor_b32 v2, s64, v49\n"
    "v_bitop3_b32 v2, v2, v51, s64 bitop3:0xf6\n"
    "v_bitop3_b32 v2, v2, v53, s64 bitop3:0xf6\n"
    "v_bitop3_b32 v2, v2, v55, s64 bitop3:0xf6\n"
    "v_bitop3_b32 v2, v2, v57, s64 bitop3:0xf6\n"
    "v_bitop3_b32 v2, v2, v59, s64 bitop3:0xf6\n"
    "v_bitop3_b32 v2, v2, v61, s64 bitop3:0xf6\n"
    "v_bitop3_b32 v2, v2, v63, s64 bitop3:0xf6\n"
    "v_bitop3_b32 v2, v2, v65, s64 bitop3:0xf6\n"
    "v_bitop3_b32 v2, v2, v67, s64 bitop3:0xf6\n"
    "v_bitop3_b32 v2, v2, v69, s64 bitop3:0xf6\n"
    "v_bitop3_b32 v2, v2, v71, s64 bitop3:0xf6\n"
    "v_bitop3_b32 v2, v2, v73, s64 bitop3:0xf6\n"
    "v_bitop3_b32 v2, v2, v75, s64 bitop3:0xf6\n"
    "v_bitop3_b32 v2, v2, v77, s64 bitop3:0xf6\n"
    "v_bitop3_b32 v2, v2, v79, s64 bitop3:0xf6\n"
    "v_cmp_ne_u32 vcc, 0, v2\n"
    "s_cbranch_vccnz .Lh3_slow_19\n"
    ".Lh3_back_20:\n"
    "buffer_load_dwordx4 v[28:31], v5, s[28:31], s41 offen sc1\n"
    "buffer_load_dwordx4 v[32:35], v5, s[28:31], s42 offen sc1\n"
    "s_waitcnt vmcnt(24)\n"
    "ds_read_b64_tr_b16 v[194:195], v112 offset:14336\n"
    "ds_read_b64_tr_b16 v[198:199], v113 offset:14336\n"
    "ds_read_b64_tr_b16 v[202:203], v114 offset:14336\n"
    "ds_read_b64_tr_b16 v[206:207], v115 offset:14336\n"
    "ds_read_b64_tr_b16 v[208:209], v112 offset:16384\n"
    "ds_read_b64_tr_b16 v[212:213], v113 offset:16384\n"
    "ds_read_b64_tr_b16 v[216:217], v114 offset:16384\n"
    "ds_read_b64_tr_b16 v[220:221], v115 offset:16384\n"
    "ds_read_b64_tr_b16 v[210:211], v112 offset:18432\n"
    "ds_read_b64_tr_b16 v[214:215], v113 offset:18432\n"
    "ds_read_b64_tr_b16 v[218:219], v114 offset:18432\n"
    "ds_read_b64_tr_b16 v[222:223], v115 offset:18432\n"
    "ds_read_b64_tr_b16 v[224:225], v112 offset:20480\n"
    "ds_read_b64_tr_b16 v[228:229], v113 offset:20480\n"
    "ds_read_b64_tr_b16 v[232:233], v114 offset:20480\n"
    "ds_read_b64_tr_b16 v[236:237], v115 offset:20480\n"
    "ds_read_b64_tr_b16 v[226:227], v112 offset:22528\n"
    "ds_read_b64_tr_b16 v[230:231], v113 offset:22528\n"
    "ds_read_b64_tr_b16 v[234:235], v114 offset:22528\n"
    "ds_read_b64_tr_b16 v[238:239], v115 offset:22528\n"
    "ds_read_b64_tr_b16 v[240:241], v112 offset:24576\n"
    "ds_read_b64_tr_b16 v[244:245], v113 offset:24576\n"
    "ds_read_b64_tr_b16 v[248:249], v114 offset:24576\n"
    "ds_read_b64_tr_b16 v[252:253], v115 offset:24576\n"
    "ds_read_b64_tr_b16 v[242:243], v112 offset:26624\n"
    "ds_read_b64_tr_b16 v[246:247], v113 offset:26624\n"
    "ds_read_b64_tr_b16 v[250:251], v114 offset:26624\n"
    "ds_read_b64_tr_b16 v[254:255], v115 offset:26624\n"
    "s_waitcnt lgkmcnt(0)\n"
    "s_add_u32 m0, s63, 14336\n"
    "s_mov_b32 s67, 3\n"
    ".Lh3_dma_21:\n"
    "s_waitcnt vmcnt(8)\n"
    "buffer_load_dwordx4 v14, s[48:51], s38 offen lds\n"
    "s_addk_i32 m0, 0x400\n"
    "s_addk_i32 s38, 14336\n"
    "buffer_load_dwordx4 v17, s[48:51], s38 offen lds\n"
    "s_addk_i32 m0, 0x400\n"
    "s_addk_i32 s38, 14336\n"
    "s_cmp_eq_u32 s38, s37\n"
    "s_cselect_b32 s38, s36, s38\n"
    "s_sub_u32 s67, s67, 1\n"
    "s_cmp_lg_u32 s67, 0\n"
    "s_cbranch_scc1 .Lh3_dma_21\n"
    "s_waitcnt vmcnt(6)\n"
    "v_lshlrev_b32 v80, 16, v28\n"
    "v_sub_f32 v96, v80, v48\n"
    "v_mul_f32 v96, v96, v50\n"
    "v_and_b32 v81, s15, v28\n"
    "v_sub_f32 v97, v81, v52\n"
    "v_mul_f32 v97, v97, v54\n"
    "v_lshlrev_b32 v82, 16, v29\n"
    "v_sub_f32 v98, v82, v56\n"
    "v_mul_f32 v98, v98, v58\n"
    "v_and_b32 v83, s15, v29\n"
    "v_sub_f32 v99, v83, v60\n"
    "v_mul_f32 v99, v99, v62\n"
    "v_lshlrev_b32 v84, 16, v30\n"
    "v_sub_f32 v100, v84, v64\n"
    "v_mul_f32 v100, v100, v66\n"
    "v_and_b32 v85, s15, v30\n"
    "v_sub_f32 v101, v85, v68\n"
    "v_mul_f32 v101, v101, v70\n"
    "v_lshlrev_b32 v86, 16, v31\n"
    "v_sub_f32 v102, v86, v72\n"
    "v_mul_f32 v102, v102, v74\n"
    "v_and_b32 v87, s15, v31\n"
    "v_sub_f32 v103, v87, v76\n"
    "v_mul_f32 v103, v103, v78\n"
    "v_lshlrev_b32 v88, 16, v32\n"
    "v_sub_f32 v104, v88, v48\n"
    "v_mul_f32 v104, v104, v50\n"
    "v_and_b32 v89, s15, v32\n"
    "v_sub_f32 v105, v89, v52\n"
    "v_mul_f32 v105, v105, v54\n"
    "v_lshlrev_b32 v90, 16, v33\n"
    "v_sub_f32 v106, v90, v56\n"
    "v_mul_f32 v106, v106, v58\n"
    "v_and_b32 v91, s15, v33\n"
    "v_sub_f32 v107, v91, v60\n"
    "v_mul_f32 v107, v107, v62\n"
    "v_lshlrev_b32 v92, 16, v34\n"
    "v_sub_f32 v108, v92, v64\n"
    "v_mul_f32 v108, v108, v66\n"
    "v_and_b32 v93, s15, v34\n"
    "v_sub_f32 v109, v93, v68\n"
    "v_mul_f32 v109, v109, v70\n"
    "v_lshlrev_b32 v94, 16, v35\n"
    "v_sub_f32 v110, v94, v72\n"
    "v_mul_f32 v110, v110, v74\n"
    "v_and_b32 v95, s15, v35\n"
    "v_sub_f32 v111, v95, v76\n"
    "v_mul_f32 v111, v111, v78\n"
    "v_bfe_u32 v2, v1, 3, 1\n"
    "v_lshlrev_b32 v3, 1, v1\n"
    "v_xor_b32 v2, v2, v3\n"
    "s_lshl_b32 s56, s13, 11\n"
    "s_add_u32 s56, s56, 131072\n"
    "v_lshl_add_u32 v6, v2, 4, s56\n"
    "v_xor_b32 v7, 16, v6\n"
    "s_setprio 3\n"
    "s_barrier\n"
    "ds_read_b128 v[48:51], v6 offset:0\n"
    "ds_read_b128 v[52:55], v7 offset:0\n"
    "ds_read_b128 v[56:59], v6 offset:8192\n"
    "ds_read_b128 v[60:63], v7 offset:8192\n"
    "ds_read_b128 v[64:67], v6 offset:16384\n"
    "ds_read_b128 v[68:71], v7 offset:16384\n"
    "ds_read_b128 v[72:75], v6 offset:24576\n"
    "ds_read_b128 v[76:79], v7 offset:24576\n"
    "s_waitcnt lgkmcnt(4)\n"
    "v_pk_add_f32 v[48:49], v[48:49], v[56:57]\n"
    "v_pk_add_f32 v[50:51], v[50:51], v[58:59]\n"
    "v_pk_add_f32 v[52:53], v[52:53], v[60:61]\n"
    "v_pk_add_f32 v[54:55], v[54:55], v[62:63]\n"
    "v_pk_fma_f32 v[48:49], v[48:49], v[96:97], v[80:81]\n"
    "v_pk_fma_f32 v[50:51], v[50:51], v[98:99], v[82:83]\n"
    "v_pk_fma_f32 v[52:53], v[52:53], v[100:101], v[84:85]\n"
    "v_pk_fma_f32 v[54:55], v[54:55], v[102:103], v[86:87]\n"
    "v_cvt_pk_bf16_f32 v28, v48, v49\n"
    "v_cvt_pk_bf16_f32 v29, v50, v51\n"
    "v_cvt_pk_bf16_f32 v30, v52, v53\n"
    "v_cvt_pk_bf16_f32 v31, v54, v55\n"
    "buffer_store_dwordx4 v[28:31], v12, s[28:31], s60 offen offset:0 sc1\n"
    "s_waitcnt lgkmcnt(0)\n"
    "v_pk_add_f32 v[64:65], v[64:65], v[72:73]\n"
    "v_pk_add_f32 v[66:67], v[66:67], v[74:75]\n"
    "v_pk_add_f32 v[68:69], v[68:69], v[76:77]\n"
    "v_pk_add_f32 v[70:71], v[70:71], v[78:79]\n"
    "v_pk_fma_f32 v[64:65], v[64:65], v[104:105], v[88:89]\n"
    "v_pk_fma_f32 v[66:67], v[66:67], v[106:107], v[90:91]\n"
    "v_pk_fma_f32 v[68:69], v[68:69], v[108:109], v[92:93]\n"
    "v_pk_fma_f32 v[70:71], v[70:71], v[110:111], v[94:95]\n"
    "v_cvt_pk_bf16_f32 v32, v64, v65\n"
    "v_cvt_pk_bf16_f32 v33, v66, v67\n"
    "v_cvt_pk_bf16_f32 v34, v68, v69\n"
    "v_cvt_pk_bf16_f32 v35, v70, v71\n"
    "buffer_store_dwordx4 v[32:35], v12, s[28:31], s60 offen offset:128 sc1\n"
    "s_setprio 0\n"
    "s_add_u32 m0, s63, 20480\n"
    "s_mov_b32 s67, 4\n"
    ".Lh3_dma_22:\n"
    "s_waitcnt vmcnt(8)\n"
    "buffer_load_dwordx4 v14, s[48:51], s38 offen lds\n"
    "s_addk_i32 m0, 0x400\n"
    "s_addk_i32 s38, 14336\n"
    "buffer_load_dwordx4 v17, s[48:51], s38 offen lds\n"
    "s_addk_i32 m0, 0x400\n"
    "s_addk_i32 s38, 14336\n"
    "s_cmp_eq_u32 s38, s37\n"
    "s_cselect_b32 s38, s36, s38\n"
    "s_sub_u32 s67, s67, 1\n"
    "s_cmp_lg_u32 s67, 0\n"
    "s_cbranch_scc1 .Lh3_dma_22\n"
    "s_lshr_b32 s59, s5, 2\n"
    "s_mul_i32 s59, s59, 0\n"
    "s_lshr_b32 s59, s59, 4\n"
    "s_lshr_b32 s57, s4, 1\n"
    "s_add_u32 s59, s59, s57\n"
    "s_and_b32 s59, s59, 15\n"
    "s_lshl_b32 s63, s13, 12\n"
    "s_lshl_b32 s58, s9, 17\n"
    "s_add_u32 s58, s58, 4292608\n"
    "s_lshl_b32 s59, s59, 12\n"
    "s_add_u32 s59, s59, s63\n"
    "s_lshr_b32 s56, s11, 1\n"
    "s_lshl_b32 s56, s56, 2\n"
    "s_and_b32 s57, s11, 1\n"
    "s_add_u32 s56, s56, s57\n"
    "s_mul_i32 s56, s56, 56\n"
    "s_mul_i32 s57, s13, 14\n"
    "s_add_u32 s56, s56, s57\n"
    "s_lshl_b32 s56, s56, 11\n"
    "s_add_u32 s56, s56, 6389760\n"
    "v_add_u32 v16, s56, v5\n"
    "v_add_u32 v17, 229376, v16\n"
    "s_lshr_b32 s56, s11, 1\n"
    "s_mul_i32 s56, s56, 56\n"
    "s_add_u32 s56, s56, s57\n"
    "s_lshl_b32 s56, s56, 7\n"
    "s_add_u32 s56, s56, 13860864\n"
    "s_add_u32 s57, s56, 114688\n"
    "v_min_u32 v27, 13, v1\n"
    "v_lshlrev_b32 v27, 7, v27\n"
    "v_and_b32 v2, 15, v1\n"
    "v_lshrrev_b32 v3, 4, v1\n"
    "v_and_b32 v6, 7, v1\n"
    "v_xor_b32 v3, v3, v6\n"
    "v_lshlrev_b32 v3, 4, v3\n"
    "v_lshl_or_b32 v2, v2, 8, v3\n"
    "v_add_u32 v2, s63, v2\n"
    "v_xor_b32 v3, 64, v2\n"
    "v_add_u32 v4, s63, v5\n"
    "v_add_u32 v18, 0x10000, v4\n"
    "v_add_u32 v8, s63, v8\n"
    "v_add_u32 v12, 0x10000, v8\n"
    "v_add_u32 v20, 0x20000, v8\n"
    "v_add_u32 v9, s63, v9\n"
    "v_add_u32 v7, 0x10000, v9\n"
    "v_add_u32 v21, 0x20000, v9\n"
    "v_add_u32 v10, s63, v10\n"
    "v_add_u32 v14, 0x10000, v10\n"
    "v_add_u32 v22, 0x20000, v10\n"
    "v_add_u32 v11, s63, v11\n"
    "v_add_u32 v15, 0x10000, v11\n"
    "v_add_u32 v23, 0x20000, v11\n"
    "s_cmp_ge_u32 s5, 28\n"
    "s_cbranch_scc1 .Lh3_B_spare\n"
    "s_waitcnt vmcnt(0)\n"
    "s_barrier\n"
    "s_waitcnt vmcnt(0) lgkmcnt(0)\n"
    "s_barrier\n"
    "s_setprio 0\n"
    "s_add_u32 m0, s63, 0\n"
    "s_add_u32 s66, s59, 0\n"
    "s_and_b32 s66, s66, 0xffff\n"
    "s_add_u32 s66, s66, s58\n"
    "s_waitcnt vmcnt(7)\n"
    "buffer_load_dwordx4 v5, s[28:31], s66 offen offset:0 sc1 lds\n"
    "s_waitcnt vmcnt(7)\n"
    "buffer_load_dwordx4 v5, s[28:31], s66 offen offset:1024 sc1 lds\n"
    "s_waitcnt vmcnt(7)\n"
    "buffer_load_dwordx4 v5, s[28:31], s66 offen offset:2048 sc1 lds\n"
    "s_waitcnt vmcnt(7)\n"
    "buffer_load_dwordx4 v5, s[28:31], s66 offen offset:3072 sc1 lds\n"
    "s_add_u32 m0, s63, 16384\n"
    "s_add_u32 s66, s59, 16384\n"
    "s_and_b32 s66, s66, 0xffff\n"
    "s_add_u32 s66, s66, s58\n"
    "s_waitcnt vmcnt(7)\n"
    "buffer_load_dwordx4 v5, s[28:31], s66 offen offset:0 sc1 lds\n"
    "s_waitcnt vmcnt(7)\n"
    "buffer_load_dwordx4 v5, s[28:31], s66 offen offset:1024 sc1 lds\n"
    "s_waitcnt vmcnt(7)\n"
    "buffer_load_dwordx4 v5, s[28:31], s66 offen offset:2048 sc1 lds\n"
    "s_waitcnt vmcnt(7)\n"
    "buffer_load_dwordx4 v5, s[28:31], s66 offen offset:3072 sc1 lds\n"
    "s_setprio 3\n"
    "s_waitcnt vmcnt(4)\n"
    "ds_read_b128 v[80:83], v4 offset:0\n"
    "ds_read_b128 v[84:87], v4 offset:1024\n"
    "ds_read_b128 v[88:91], v4 offset:2048\n"
    "ds_read_b128 v[92:95], v4 offset:3072\n"
    "s_waitcnt lgkmcnt(0)\n"
    "v_max3_u32 v80, v80, v81, v82\n"
    "v_max3_u32 v83, v83, v84, v85\n"
    "v_max3_u32 v86, v86, v87, v88\n"
    "v_max3_u32 v89, v89, v90, v91\n"
    "v_max3_u32 v92, v92, v93, v94\n"
    "v_max3_u32 v80, v80, v83, v86\n"
    "v_max3_u32 v89, v89, v92, v95\n"
    "v_max_u32 v80, v80, v89\n"
    "v_cmp_eq_u32 vcc, -1, v80\n"
    "s_cbranch_vccnz .Lh3_slow_23\n"
    ".Lh3_back_24:\n"
    "s_barrier\n"
    "s_setprio 0\n"
    "s_add_u32 m0, s63, 32768\n"
    "s_add_u32 s66, s59, 32768\n"
    "s_and_b32 s66, s66, 0xffff\n"
    "s_add_u32 s66, s66, s58\n"
    "s_waitcnt vmcnt(7)\n"
    "buffer_load_dwordx4 v5, s[28:31], s66 offen offset:0 sc1 lds\n"
    "s_waitcnt vmcnt(7)\n"
    "buffer_load_dwordx4 v5, s[28:31], s66 offen offset:1024 sc1 lds\n"
    "s_waitcnt vmcnt(7)\n"
    "buffer_load_dwordx4 v5, s[28:31], s66 offen offset:2048 sc1 lds\n"
    "s_waitcnt vmcnt(7)\n"
    "buffer_load_dwordx4 v5, s[28:31], s66 offen offset:3072 sc1 lds\n"
    "s_add_u32 m0, s63, 49152\n"
    "s_add_u32 s66, s59, 49152\n"
    "s_and_b32 s66, s66, 0xffff\n"
    "s_add_u32 s66, s66, s58\n"
    "s_waitcnt vmcnt(7)\n"
    "buffer_load_dwordx4 v5, s[28:31], s66 offen offset:0 sc1 lds\n"
    "s_waitcnt vmcnt(7)\n"
    "buffer_load_dwordx4 v5, s[28:31], s66 offen offset:1024 sc1 lds\n"
    "s_waitcnt vmcnt(7)\n"
    "buffer_load_dwordx4 v5, s[28:31], s66 offen offset:2048 sc1 lds\n"
    "s_waitcnt vmcnt(7)\n"
    "buffer_load_dwordx4 v5, s[28:31], s66 offen offset:3072 sc1 lds\n"
    "s_setprio 3\n"
    "s_waitcnt vmcnt(8)\n"
    "ds_read_b128 v[96:99], v4 offset:16384\n"
    "ds_read_b128 v[100:103], v4 offset:17408\n"
    "ds_read_b128 v[104:107], v4 offset:18432\n"
    "ds_read_b128 v[108:111], v4 offset:19456\n"
    "s_waitcnt lgkmcnt(0)\n"
    "v_max3_u32 v96, v96, v97, v98\n"
    "v_max3_u32 v99, v99, v100, v101\n"
    "v_max3_u32 v102, v102, v103, v104\n"
    "v_max3_u32 v105, v105, v106, v107\n"
    "v_max3_u32 v108, v108, v109, v110\n"
    "v_max3_u32 v96, v96, v99, v102\n"
    "v_max3_u32 v105, v105, v108, v111\n"
    "v_max_u32 v96, v96, v105\n"
    "v_cmp_eq_u32 vcc, -1, v96\n"
    "s_cbranch_vccnz .Lh3_slow_25\n"
    ".Lh3_back_26:\n"
    "s_barrier\n"
    "s_setprio 0\n"
    "s_add_u32 m0, s63, 65536\n"
    "s_add_u32 s66, s59, 0\n"
    "s_and_b32 s66, s66, 0xffff\n"
    "s_add_u32 s66, s66, s58\n"
    "s_add_u32 s66, s66, 65536\n"
    "s_waitcnt vmcnt(7)\n"
    "buffer_load_dwordx4 v5, s[28:31], s66 offen offset:0 sc1 lds\n"
    "s_waitcnt vmcnt(7)\n"
    "buffer_load_dwordx4 v5, s[28:31], s66 offen offset:1024 sc1 lds\n"
    "s_waitcnt vmcnt(7)\n"
    "buffer_load_dwordx4 v5, s[28:31], s66 offen offset:2048 sc1 lds\n"
    "s_waitcnt vmcnt(7)\n"
    "buffer_load_dwordx4 v5, s[28:31], s66 offen offset:3072 sc1 lds\n"
    "s_setprio 3\n"
    "s_waitcnt vmcnt(8)\n"
    "ds_read_b128 v[112:115], v4 offset:32768\n"
    "ds_read_b128 v[116:119], v4 offset:33792\n"
    "ds_read_b128 v[120:123], v4 offset:34816\n"
    "ds_read_b128 v[124:127], v4 offset:35840\n"
    "s_waitcnt lgkmcnt(0)\n"
    "v_max3_u32 v112, v112, v113, v114\n"
    "v_max3_u32 v115, v115, v116, v117\n"
    "v_max3_u32 v118, v118, v119, v120\n"
    "v_max3_u32 v121, v121, v122, v123\n"
    "v_max3_u32 v124, v124, v125, v126\n"
    "v_max3_u32 v112, v112, v115, v118\n"
    "v_max3_u32 v121, v121, v124, v127\n"
    "v_max_u32 v112, v112, v121\n"
    "v_cmp_eq_u32 vcc, -1, v112\n"
    "s_cbranch_vccnz .Lh3_slow_27\n"
    ".Lh3_back_28:\n"
    "s_setprio 0\n"
    "s_add_u32 m0, s63, 81920\n"
    "s_add_u32 s66, s59, 16384\n"
    "s_and_b32 s66, s66, 0xffff\n"
    "s_add_u32 s66, s66, s58\n"
    "s_add_u32 s66, s66, 65536\n"
    "s_waitcnt vmcnt(7)\n"
    "buffer_load_dwordx4 v5, s[28:31], s66 offen offset:0 sc1 lds\n"
    "s_waitcnt vmcnt(7)\n"
    "buffer_load_dwordx4 v5, s[28:31], s66 offen offset:1024 sc1 lds\n"
    "s_waitcnt vmcnt(7)\n"
    "buffer_load_dwordx4 v5, s[28:31], s66 offen offset:2048 sc1 lds\n"
    "s_waitcnt vmcnt(7)\n"
    "buffer_load_dwordx4 v5, s[28:31], s66 offen offset:3072 sc1 lds\n"
    "s_barrier\n"
    "s_setprio 3\n"
    "s_waitcnt vmcnt(8)\n"
    "ds_read_b128 v[80:83], v4 offset:49152\n"
    "ds_read_b128 v[84:87], v4 offset:50176\n"
    "ds_read_b128 v[88:91], v4 offset:51200\n"
    "ds_read_b128 v[92:95], v4 offset:52224\n"
    "s_waitcnt lgkmcnt(0)\n"
    "v_max3_u32 v80, v80, v81, v82\n"
    "v_max3_u32 v83, v83, v84, v85\n"
    "v_max3_u32 v86, v86, v87, v88\n"
    "v_max3_u32 v89, v89, v90, v91\n"
    "v_max3_u32 v92, v92, v93, v94\n"
    "v_max3_u32 v80, v80, v83, v86\n"
    "v_max3_u32 v89, v89, v92, v95\n"
    "v_max_u32 v80, v80, v89\n"
    "v_cmp_eq_u32 vcc, -1, v80\n"
    "s_cbranch_vccnz .Lh3_slow_29\n"
    ".Lh3_back_30:\n"
    "s_setprio 0\n"
    "s_add_u32 m0, s63, 98304\n"
    "s_add_u32 s66, s59, 32768\n"
    "s_and_b32 s66, s66, 0xffff\n"
    "s_add_u32 s66, s66, s58\n"
    "s_add_u32 s66, s66, 65536\n"
    "s_waitcnt vmcnt(7)\n"
    "buffer_load_dwordx4 v5, s[28:31], s66 offen offset:0 sc1 lds\n"
    "s_waitcnt vmcnt(7)\n"
    "buffer_load_dwordx4 v5, s[28:31], s66 offen offset:1024 sc1 lds\n"
    "s_waitcnt vmcnt(7)\n"
    "buffer_load_dwordx4 v5, s[28:31], s66 offen offset:2048 sc1 lds\n"
    "s_waitcnt vmcnt(7)\n"
    "buffer_load_dwordx4 v5, s[28:31], s66 offen offset:3072 sc1 lds\n"
    "s_add_u32 m0, s63, 114688\n"
    "s_add_u32 s66, s59, 49152\n"
    "s_and_b32 s66, s66, 0xffff\n"
    "s_add_u32 s66, s66, s58\n"
    "s_add_u32 s66, s66, 65536\n"
    "s_waitcnt vmcnt(7)\n"
    "buffer_load_dwordx4 v5, s[28:31], s66 offen offset:0 sc1 lds\n"
    "s_waitcnt vmcnt(7)\n"
    "buffer_load_dwordx4 v5, s[28:31], s66 offen offset:1024 sc1 lds\n"
    "s_waitcnt vmcnt(7)\n"
    "buffer_load_dwordx4 v5, s[28:31], s66 offen offset:2048 sc1 lds\n"
    "s_waitcnt vmcnt(7)\n"
    "buffer_load_dwordx4 v5, s[28:31], s66 offen offset:3072 sc1 lds\n"
    "s_barrier\n"
    "s_add_u32 m0, s63, 131072\n"
    "s_lshl4_add_u32 s62, s23, s54\n"
    "s_waitcnt vmcnt(7)\n"
    "buffer_load_dwordx4 v13, s[44:47], s62 offen lds\n"
    "s_addk_i32 m0, 0x400\n"
    "s_addk_i32 s62, 8192\n"
    "s_waitcnt vmcnt(7)\n"
    "buffer_load_dwordx4 v13, s[44:47], s62 offen lds\n"
    "s_addk_i32 m0, 0x400\n"
    "s_addk_i32 s62, 8192\n"
    "s_waitcnt vmcnt(7)\n"
    "buffer_load_dwordx4 v13, s[44:47], s62 offen lds\n"
    "s_addk_i32 m0, 0x400\n"
    "s_addk_i32 s62, 8192\n"
    "s_waitcnt vmcnt(7)\n"
    "buffer_load_dwordx4 v13, s[44:47], s62 offen lds\n"
    "s_addk_i32 m0, 0x400\n"
    "s_addk_i32 s62, 8192\n"
    "s_setprio 3\n"
    "s_waitcnt vmcnt(16)\n"
    "ds_read_b128 v[96:99], v18 offset:0\n"
    "ds_read_b128 v[100:103], v18 offset:1024\n"
    "ds_read_b128 v[104:107], v18 offset:2048\n"
    "ds_read_b128 v[108:111], v18 offset:3072\n"
    "s_waitcnt vmcnt(12)\n"
    "ds_read_b128 v[112:115], v18 offset:16384\n"
    "ds_read_b128 v[116:119], v18 offset:17408\n"
    "ds_read_b128 v[120:123], v18 offset:18432\n"
    "ds_read_b128 v[124:127], v18 offset:19456\n"
    "s_waitcnt lgkmcnt(4)\n"
    "v_max3_u32 v96, v96, v97, v98\n"
    "v_max3_u32 v99, v99, v100, v101\n"
    "v_max3_u32 v102, v102, v103, v104\n"
    "v_max3_u32 v105, v105, v106, v107\n"
    "v_max3_u32 v108, v108, v109, v110\n"
    "v_max3_u32 v96, v96, v99, v102\n"
    "v_max3_u32 v105, v105, v108, v111\n"
    "v_max_u32 v96, v96, v105\n"
    "v_cmp_eq_u32 vcc, -1, v96\n"
    "s_cbranch_vccnz .Lh3_slow_31\n"
    ".Lh3_back_32:\n"
    "s_waitcnt lgkmcnt(0)\n"
    "v_max3_u32 v112, v112, v113, v114\n"
    "v_max3_u32 v115, v115, v116, v117\n"
    "v_max3_u32 v118, v118, v119, v120\n"
    "v_max3_u32 v121, v121, v122, v123\n"
    "v_max3_u32 v124, v124, v125, v126\n"
    "v_max3_u32 v112, v112, v115, v118\n"
    "v_max3_u32 v121, v121, v124, v127\n"
    "v_max_u32 v112, v112, v121\n"
    "v_cmp_eq_u32 vcc, -1, v112\n"
    "s_cbranch_vccnz .Lh3_slow_33\n"
    ".Lh3_back_34:\n"
    "s_barrier\n"
    "s_setprio 0\n"
    "s_add_u32 m0, s63, 0\n"
    "s_lshl4_add_u32 s62, s24, s54\n"
    "s_waitcnt vmcnt(7)\n"
    "buffer_load_dwordx4 v13, s[44:47], s62 offen lds\n"
    "s_addk_i32 m0, 0x400\n"
    "s_addk_i32 s62, 8192\n"
    "s_waitcnt vmcnt(7)\n"
    "buffer_load_dwordx4 v13, s[44:47], s62 offen lds\n"
    "s_addk_i32 m0, 0x400\n"
    "s_addk_i32 s62, 8192\n"
    "s_waitcnt vmcnt(7)\n"
    "buffer_load_dwordx4 v13, s[44:47], s62 offen lds\n"
    "s_addk_i32 m0, 0x400\n"
    "s_addk_i32 s62, 8192\n"
    "s_waitcnt vmcnt(7)\n"
    "buffer_load_dwordx4 v13, s[44:47], s62 offen lds\n"
    "s_addk_i32 m0, 0x400\n"
    "s_addk_i32 s62, 8192\n"
    "s_setprio 3\n"
    "s_waitcnt vmcnt(12)\n"
    "ds_read_b128 v[80:83], v18 offset:32768\n"
    "ds_read_b128 v[84:87], v18 offset:33792\n"
    "ds_read_b128 v[88:91], v18 offset:34816\n"
    "ds_read_b128 v[92:95], v18 offset:35840\n"
    "s_waitcnt vmcnt(8)\n"
    "ds_read_b128 v[96:99], v18 offset:49152\n"
    "ds_read_b128 v[100:103], v18 offset:50176\n"
    "ds_read_b128 v[104:107], v18 offset:51200\n"
    "ds_read_b128 v[108:111], v18 offset:52224\n"
    "s_waitcnt lgkmcnt(4)\n"
    "v_max3_u32 v80, v80, v81, v82\n"
    "v_max3_u32 v83, v83, v84, v85\n"
    "v_max3_u32 v86, v86, v87, v88\n"
    "v_max3_u32 v89, v89, v90, v91\n"
    "v_max3_u32 v92, v92, v93, v94\n"
    "v_max3_u32 v80, v80, v83, v86\n"
    "v_max3_u32 v89, v89, v92, v95\n"
    "v_max_u32 v80, v80, v89\n"
    "v_cmp_eq_u32 vcc, -1, v80\n"
    "s_cbranch_vccnz .Lh3_slow_35\n"
    ".Lh3_back_36:\n"
    "s_waitcnt lgkmcnt(0)\n"
    "v_max3_u32 v96, v96, v97, v98\n"
    "v_max3_u32 v99, v99, v100, v101\n"
    "v_max3_u32 v102, v102, v103, v104\n"
    "v_max3_u32 v105, v105, v106, v107\n"
    "v_max3_u32 v108, v108, v109, v110\n"
    "v_max3_u32 v96, v96, v99, v102\n"
    "v_max3_u32 v105, v105, v108, v111\n"
    "v_max_u32 v96, v96, v105\n"
    "v_cmp_eq_u32 vcc, -1, v96\n"
    "s_cbranch_vccnz .Lh3_slow_37\n"
    ".Lh3_back_38:\n"
    "s_barrier\n"
    "s_setprio 0\n"
    "s_or_b32 s66, s2, s13\n"
    "s_cmp_lg_u32 s66, 0\n"
    "s_cbranch_scc1 .Lh3_B_noE\n"
    "s_mov_b64 exec, 1\n"
    "buffer_store_dwordx2 v[24:25], off, s[28:31], 0 sc1\n"
    "s_mov_b64 exec, -1\n"
    ".Lh3_B_noE:\n"
    "s_add_u32 m0, s63, 16384\n"
    "s_lshl4_add_u32 s62, s25, s54\n"
    "s_waitcnt vmcnt(7)\n"
    "buffer_load_dwordx4 v13, s[44:47], s62 offen lds\n"
    "s_addk_i32 m0, 0x400\n"
    "s_addk_i32 s62, 8192\n"
    "s_waitcnt vmcnt(7)\n"
    "buffer_load_dwordx4 v13, s[44:47], s62 offen lds\n"
    "s_addk_i32 m0, 0x400\n"
    "s_addk_i32 s62, 8192\n"
    "s_waitcnt vmcnt(7)\n"
    "buffer_load_dwordx4 v13, s[44:47], s62 offen lds\n"
    "s_addk_i32 m0, 0x400\n"
    "s_addk_i32 s62, 8192\n"
    "s_waitcnt vmcnt(7)\n"
    "buffer_load_dwordx4 v13, s[44:47], s62 offen lds\n"
    "s_addk_i32 m0, 0x400\n"
    "s_addk_i32 s62, 8192\n"
    "s_add_u32 m0, s63, 32768\n"
    "s_lshl4_add_u32 s62, s26, s54\n"
    "s_waitcnt vmcnt(7)\n"
    "buffer_load_dwordx4 v13, s[44:47], s62 offen lds\n"
    "s_addk_i32 m0, 0x400\n"
    "s_addk_i32 s62, 8192\n"
    "s_waitcnt vmcnt(7)\n"
    "buffer_load_dwordx4 v13, s[44:47], s62 offen lds\n"
    "s_addk_i32 m0, 0x400\n"
    "s_addk_i32 s62, 8192\n"
    "s_waitcnt vmcnt(7)\n"
    "buffer_load_dwordx4 v13, s[44:47], s62 offen lds\n"
    "s_addk_i32 m0, 0x400\n"
    "s_addk_i32 s62, 8192\n"
    "s_waitcnt vmcnt(7)\n"
    "buffer_load_dwordx4 v13, s[44:47], s62 offen lds\n"
    "s_addk_i32 m0, 0x400\n"
    "s_addk_i32 s62, 8192\n"
    "s_add_u32 m0, s63, 49152\n"
    "s_lshl4_add_u32 s62, s27, s54\n"
    "s_waitcnt vmcnt(7)\n"
    "buffer_load_dwordx4 v13, s[44:47], s62 offen lds\n"
    "s_addk_i32 m0, 0x400\n"
    "s_addk_i32 s62, 8192\n"
    "s_waitcnt vmcnt(7)\n"
    "buffer_load_dwordx4 v13, s[44:47], s62 offen lds\n"
    "s_addk_i32 m0, 0x400\n"
    "s_addk_i32 s62, 8192\n"
    "s_waitcnt vmcnt(7)\n"
    "buffer_load_dwordx4 v13, s[44:47], s62 offen lds\n"
    "s_addk_i32 m0, 0x400\n"
    "s_addk_i32 s62, 8192\n"
    "s_waitcnt vmcnt(7)\n"
    "buffer_load_dwordx4 v13, s[44:47], s62 offen lds\n"
    "s_addk_i32 m0, 0x400\n"
    "s_addk_i32 s62, 8192\n"
    "s_barrier\n"
    "s_add_u32 m0, s63, 65536\n"
    "s_lshl4_add_u32 s62, s52, s54\n"
    "s_waitcnt vmcnt(7)\n"
    "buffer_load_dwordx4 v13, s[44:47], s62 offen lds\n"
    "s_addk_i32 m0, 0x400\n"
    "s_addk_i32 s62, 8192\n"
    "s_waitcnt vmcnt(7)\n"
    "buffer_load_dwordx4 v13, s[44:47], s62 offen lds\n"
    "s_addk_i32 m0, 0x400\n"
    "s_addk_i32 s62, 8192\n"
    "s_waitcnt vmcnt(7)\n"
    "buffer_load_dwordx4 v13, s[44:47], s62 offen lds\n"
    "s_addk_i32 m0, 0x400\n"
    "s_addk_i32 s62, 8192\n"
    "s_waitcnt vmcnt(7)\n"
    "buffer_load_dwordx4 v13, s[44:47], s62 offen lds\n"
    "s_addk_i32 m0, 0x400\n"
    "s_addk_i32 s62, 8192\n"
    "buffer_load_dwordx2 v[28:29], v27, s[28:31], s56 offen sc1\n"
    "s_add_u32 m0, s63, 81920\n"
    "s_lshl4_add_u32 s62, s53, s54\n"
    "s_waitcnt vmcnt(7)\n"
    "buffer_load_dwordx4 v13, s[44:47], s62 offen lds\n"
    "s_addk_i32 m0, 0x400\n"
    "s_addk_i32 s62, 8192\n"
    "s_waitcnt vmcnt(7)\n"
    "buffer_load_dwordx4 v13, s[44:47], s62 offen lds\n"
    "s_addk_i32 m0, 0x400\n"
    "s_addk_i32 s62, 8192\n"
    "s_waitcnt vmcnt(7)\n"
    "buffer_load_dwordx4 v13, s[44:47], s62 offen lds\n"
    "s_addk_i32 m0, 0x400\n"
    "s_addk_i32 s62, 8192\n"
    "s_waitcnt vmcnt(7)\n"
    "buffer_load_dwordx4 v13, s[44:47], s62 offen lds\n"
    "s_addk_i32 m0, 0x400\n"
    "s_addk_i32 s62, 8192\n"
    "s_setprio 3\n"
    ".Lh3_B_join:\n"
    "s_waitcnt vmcnt(4)\n"
    "v_xor_b32 v28, s64, v28\n"
    "v_xor_b32 v29, s65, v29\n"
    "v_or_b32 v28, v28, v29\n"
    "v_cmp_ne_u32 vcc, 0, v28\n"
    "s_cbranch_vccnz .Lh3_slow_39\n"
    ".Lh3_back_40:\n"
    "buffer_load_dwordx4 v[32:35], v16, s[28:31], s16 offen offset:0 sc1\n"
    "buffer_load_dwordx4 v[36:39], v16, s[28:31], s16 offen offset:1024 sc1\n"
    "buffer_load_dwordx4 v[40:43], v16, s[28:31], s17 offen offset:0 sc1\n"
    "buffer_load_dwordx4 v[44:47], v16, s[28:31], s17 offen offset:1024 sc1\n"
    "buffer_load_dwordx4 v[48:51], v16, s[28:31], s18 offen offset:0 sc1\n"
    "buffer_load_dwordx4 v[52:55], v16, s[28:31], s18 offen offset:1024 sc1\n"
    "buffer_load_dwordx4 v[56:59], v16, s[28:31], s19 offen offset:0 sc1\n"
    "buffer_load_dwordx4 v[60:63], v16, s[28:31], s19 offen offset:1024 sc1\n"
    "buffer_load_dwordx4 v[64:67], v16, s[28:31], s20 offen offset:0 sc1\n"
    "buffer_load_dwordx4 v[68:71], v16, s[28:31], s20 offen offset:1024 sc1\n"
    "s_waitcnt vmcnt(8)\n"
    "v_mfma_f32_16x16x32_bf16 v[80:83], v[144:147], v[32:35], 0\n"
    "buffer_load_dwordx4 v[72:75], v16, s[28:31], s21 offen offset:0 sc1\n"
    "v_mfma_f32_16x16x32_bf16 v[84:87], v[148:151], v[32:35], 0\n"
    "v_mfma_f32_16x16x32_bf16 v[88:91], v[152:155], v[32:35], 0\n"
    "v_mfma_f32_16x16x32_bf16 v[92:95], v[156:159], v[32:35], 0\n"
    "v_mfma_f32_16x16x32_bf16 v[96:99], v[144:147], v[36:39], 0\n"
    "buffer_load_dwordx4 v[76:79], v16, s[28:31], s21 offen offset:1024 sc1\n"
    "v_mfma_f32_16x16x32_bf16 v[100:103], v[148:151], v[36:39], 0\n"
    "v_mfma_f32_16x16x32_bf16 v[104:107], v[152:155], v[36:39], 0\n"
    "v_mfma_f32_16x16x32_bf16 v[108:111], v[156:159], v[36:39], 0\n"
    "s_waitcnt vmcnt(8)\n"
    "v_mfma_f32_16x16x32_bf16 v[80:83], v[160:163], v[40:43], v[80:83]\n"
    "buffer_load_dwordx4 v[32:35], v16, s[28:31], s22 offen offset:0 sc1\n"
    "v_mfma_f32_16x16x32_bf16 v[84:87], v[164:167], v[40:43], v[84:87]\n"
    "v_mfma_f32_16x16x32_bf16 v[88:91], v[168:171], v[40:43], v[88:91]\n"
    "v_mfma_f32_16x16x32_bf16 v[92:95], v[172:175], v[40:43], v[92:95]\n"
    "v_mfma_f32_16x16x32_bf16 v[96:99], v[160:163], v[44:47], v[96:99]\n"
    "buffer_load_dwordx4 v[36:39], v16, s[28:31], s22 offen offset:1024 sc1\n"
    "v_mfma_f32_16x16x32_bf16 v[100:103], v[164:167], v[44:47], v[100:103]\n"
    "v_mfma_f32_16x16x32_bf16 v[104:107], v[168:171], v[44:47], v[104:107]\n"
    "v_mfma_f32_16x16x32_bf16 v[108:111], v[172:175], v[44:47], v[108:111]\n"
    "s_waitcnt vmcnt(8)\n"
    "v_mfma_f32_16x16x32_bf16 v[80:83], v[176:179], v[48:51], v[80:83]\n"
    "buffer_load_dwordx4 v[40:43], v16, s[28:31], s23 offen offset:0 sc1\n"
    "v_mfma_f32_16x16x32_bf16 v[84:87], v[180:183], v[48:51], v[84:87]\n"
    "v_mfma_f32_16x16x32_bf16 v[88:91], v[184:187], v[48:51], v[88:91]\n"
    "v_mfma_f32_16x16x32_bf16 v[92:95], v[188:191], v[48:51], v[92:95]\n"
    "v_mfma_f32_16x16x32_bf16 v[96:99], v[176:179], v[52:55], v[96:99]\n"
    "buffer_load_dwordx4 v[44:47], v16, s[28:31], s23 offen offset:1024 sc1\n"
    "v_mfma_f32_16x16x32_bf16 v[100:103], v[180:183], v[52:55], v[100:103]\n"
    "v_mfma_f32_16x16x32_bf16 v[104:107], v[184:187], v[52:55], v[104:107]\n"
    "v_mfma_f32_16x16x32_bf16 v[108:111], v[188:191], v[52:55], v[108:111]\n"
    "s_waitcnt vmcnt(8)\n"
    "v_mfma_f32_16x16x32_bf16 v[80:83], v[192:195], v[56:59], v[80:83]\n"
    "buffer_load_dwordx4 v[48:51], v16, s[28:31], s24 offen offset:0 sc1\n"
    "v_mfma_f32_16x16x32_bf16 v[84:87], v[196:199], v[56:59], v[84:87]\n"
    "v_mfma_f32_16x16x32_bf16 v[88:91], v[200:203], v[56:59], v[88:91]\n"
    "v_mfma_f32_16x16x32_bf16 v[92:95], v[204:207], v[56:59], v[92:95]\n"
    "v_mfma_f32_16x16x32_bf16 v[96:99], v[192:195], v[60:63], v[96:99]\n"
    "buffer_load_dwordx4 v[52:55], v16, s[28:31], s24 offen offset:1024 sc1\n"
    "v_mfma_f32_16x16x32_bf16 v[100:103], v[196:199], v[60:63], v[100:103]\n"
    "v_mfma_f32_16x16x32_bf16 v[104:107], v[200:203], v[60:63], v[104:107]\n"
    "v_mfma_f32_16x16x32_bf16 v[108:111], v[204:207], v[60:63], v[108:111]\n"
    "s_waitcnt vmcnt(8)\n"
    "v_mfma_f32_16x16x32_bf16 v[80:83], v[208:211], v[64:67], v[80:83]\n"
    "buffer_load_dwordx4 v[56:59], v16, s[28:31], s25 offen offset:0 sc1\n"
    "v_mfma_f32_16x16x32_bf16 v[84:87], v[212:215], v[64:67], v[84:87]\n"
    "v_mfma_f32_16x16x32_bf16 v[88:91], v[216:219], v[64:67], v[88:91]\n"
    "v_mfma_f32_16x16x32_bf16 v[92:95], v[220:223], v[64:67], v[92:95]\n"
    "v_mfma_f32_16x16x32_bf16 v[96:99], v[208:211], v[68:71], v[96:99]\n"
    "buffer_load_dwordx4 v[60:63], v16, s[28:31], s25 offen offset:1024 sc1\n"
    "v_mfma_f32_16x16x32_bf16 v[100:103], v[212:215], v[68:71], v[100:103]\n"
    "v_mfma_f32_16x16x32_bf16 v[104:107], v[216:219], v[68:71], v[104:107]\n"
    "v_mfma_f32_16x16x32_bf16 v[108:111], v[220:223], v[68:71], v[108:111]\n"
    "s_waitcnt vmcnt(8)\n"
    "v_mfma_f32_16x16x32_bf16 v[80:83], v[224:227], v[72:75], v[80:83]\n"
    "buffer_load_dwordx4 v[64:67], v16, s[28:31], s26 offen offset:0 sc1\n"
    "v_mfma_f32_16x16x32_bf16 v[84:87], v[228:231], v[72:75], v[84:87]\n"
    "v_mfma_f32_16x16x32_bf16 v[88:91], v[232:235], v[72:75], v[88:91]\n"
    "v_mfma_f32_16x16x32_bf16 v[92:95], v[236:239], v[72:75], v[92:95]\n"
    "v_mfma_f32_16x16x32_bf16 v[96:99], v[224:227], v[76:79], v[96:99]\n"
    "buffer_load_dwordx4 v[68:71], v16, s[28:31], s26 offen offset:1024 sc1\n"
    "v_mfma_f32_16x16x32_bf16 v[100:103], v[228:231], v[76:79], v[100:103]\n"
    "v_mfma_f32_16x16x32_bf16 v[104:107], v[232:235], v[76:79], v[104:107]\n"
    "v_mfma_f32_16x16x32_bf16 v[108:111], v[236:239], v[76:79], v[108:111]\n"
    "s_waitcnt vmcnt(8)\n"
    "v_mfma_f32_16x16x32_bf16 v[80:83], v[240:243], v[32:35], v[80:83]\n"
    "ds_read_b64_tr_b16 v[128:129], v20 offset:0\n"
    "ds_read_b64_tr_b16 v[130:131], v20 offset:2048\n"
    "buffer_load_dwordx4 v[72:75], v16, s[28:31], s27 offen offset:0 sc1\n"
    "v_mfma_f32_16x16x32_bf16 v[84:87], v[244:247], v[32:35], v[84:87]\n"
    "ds_read_b64_tr_b16 v[132:133], v21 offset:0\n"
    "ds_read_b64_tr_b16 v[134:135], v21 offset:2048\n"
    "v_mfma_f32_16x16x32_bf16 v[88:91], v[248:251], v[32:35], v[88:91]\n"
    "ds_read_b64_tr_b16 v[136:137], v22 offset:0\n"
    "ds_read_b64_tr_b16 v[138:139], v22 offset:2048\n"
    "v_mfma_f32_16x16x32_bf16 v[92:95], v[252:255], v[32:35], v[92:95]\n"
    "ds_read_b64_tr_b16 v[140:141], v23 offset:0\n"
    "ds_read_b64_tr_b16 v[142:143], v23 offset:2048\n"
    "v_mfma_f32_16x16x32_bf16 v[96:99], v[240:243], v[36:39], v[96:99]\n"
    "buffer_load_dwordx4 v[76:79], v16, s[28:31], s27 offen offset:1024 sc1\n"
    "v_mfma_f32_16x16x32_bf16 v[100:103], v[244:247], v[36:39], v[100:103]\n"
    "v_mfma_f32_16x16x32_bf16 v[104:107], v[248:251], v[36:39], v[104:107]\n"
    "v_mfma_f32_16x16x32_bf16 v[108:111], v[252:255], v[36:39], v[108:111]\n"
    "s_waitcnt vmcnt(8)\n"
    "s_waitcnt lgkmcnt(0)\n"
    "buffer_load_dwordx2 v[28:29], v27, s[28:31], s57 offen sc1\n"
    "v_mfma_f32_16x16x32_bf16 v[80:83], v[128:131], v[40:43], v[80:83]\n"
    "ds_read_b64_tr_b16 v[112:113], v8 offset:0\n"
    "ds_read_b64_tr_b16 v[114:115], v8 offset:2048\n"
    "buffer_load_dwordx4 v[32:35], v16, s[28:31], s52 offen offset:0 sc1\n"
    "v_mfma_f32_16x16x32_bf16 v[84:87], v[132:135], v[40:43], v[84:87]\n"
    "ds_read_b64_tr_b16 v[116:117], v9 offset:0\n"
    "ds_read_b64_tr_b16 v[118:119], v9 offset:2048\n"
    "v_mfma_f32_16x16x32_bf16 v[88:91], v[136:139], v[40:43], v[88:91]\n"
    "ds_read_b64_tr_b16 v[120:121], v10 offset:0\n"
    "ds_read_b64_tr_b16 v[122:123], v10 offset:2048\n"
    "v_mfma_f32_16x16x32_bf16 v[92:95], v[140:143], v[40:43], v[92:95]\n"
    "ds_read_b64_tr_b16 v[124:125], v11 offset:0\n"
    "ds_read_b64_tr_b16 v[126:127], v11 offset:2048\n"
    "v_mfma_f32_16x16x32_bf16 v[96:99], v[128:131], v[44:47], v[96:99]\n"
    "buffer_load_dwordx4 v[36:39], v16, s[28:31], s52 offen offset:1024 sc1\n"
    "v_mfma_f32_16x16x32_bf16 v[100:103], v[132:135], v[44:47], v[100:103]\n"
    "v_mfma_f32_16x16x32_bf16 v[104:107], v[136:139], v[44:47], v[104:107]\n"
    "v_mfma_f32_16x16x32_bf16 v[108:111], v[140:143], v[44:47], v[108:111]\n"
    "s_waitcnt vmcnt(9)\n"
    "s_waitcnt lgkmcnt(0)\n"
    "v_mfma_f32_16x16x32_bf16 v[80:83], v[112:115], v[48:51], v[80:83]\n"
    "ds_read_b64_tr_b16 v[128:129], v8 offset:16384\n"
    "ds_read_b64_tr_b16 v[130:131], v8 offset:18432\n"
    "buffer_load_dwordx4 v[40:43], v16, s[28:31], s53 offen offset:0 sc1\n"
    "v_mfma_f32_16x16x32_bf16 v[84:87], v[116:119], v[48:51], v[84:87]\n"
    "ds_read_b64_tr_b16 v[132:133], v9 offset:16384\n"
    "ds_read_b64_tr_b16 v[134:135], v9 offset:18432\n"
    "v_mfma_f32_16x16x32_bf16 v[88:91], v[120:123], v[48:51], v[88:91]\n"
    "ds_read_b64_tr_b16 v[136:137], v10 offset:16384\n"
    "ds_read_b64_tr_b16 v[138:139], v10 offset:18432\n"
    "v_mfma_f32_16x16x32_bf16 v[92:95], v[124:127], v[48:51], v[92:95]\n"
    "ds_read_b64_tr_b16 v[140:141], v11 offset:16384\n"
    "ds_read_b64_tr_b16 v[142:143], v11 offset:18432\n"
    "v_mfma_f32_16x16x32_bf16 v[96:99], v[112:115], v[52:55], v[96:99]\n"
    "buffer_load_dwordx4 v[44:47], v16, s[28:31], s53 offen offset:1024 sc1\n"
    "v_mfma_f32_16x16x32_bf16 v[100:103], v[116:119], v[52:55], v[100:103]\n"
    "v_mfma_f32_16x16x32_bf16 v[104:107], v[120:123], v[52:55], v[104:107]\n"
    "v_mfma_f32_16x16x32_bf16 v[108:111], v[124:127], v[52:55], v[108:111]\n"
    "s_waitcnt vmcnt(9)\n"
    "s_waitcnt lgkmcnt(0)\n"
    "s_waitcnt vmcnt(4)\n"
    "v_xor_b32 v28, s64, v28\n"
    "v_xor_b32 v29, s65, v29\n"
    "v_or_b32 v28, v28, v29\n"
    "v_cmp_ne_u32 vcc, 0, v28\n"
    "s_cbranch_vccnz .Lh3_slow_41\n"
    ".Lh3_back_42:\n"
    "v_mfma_f32_16x16x32_bf16 v[80:83], v[128:131], v[56:59], v[80:83]\n"
    "ds_read_b64_tr_b16 v[112:113], v8 offset:32768\n"
    "ds_read_b64_tr_b16 v[114:115], v8 offset:34816\n"
    "buffer_load_dwordx4 v[48:51], v17, s[28:31], s16 offen offset:0 sc1\n"
    "v_mfma_f32_16x16x32_bf16 v[84:87], v[132:135], v[56:59], v[84:87]\n"
    "ds_read_b64_tr_b16 v[116:117], v9 offset:32768\n"
    "ds_read_b64_tr_b16 v[118:119], v9 offset:34816\n"
    "v_mfma_f32_16x16x32_bf16 v[88:91], v[136:139], v[56:59], v[88:91]\n"
    "ds_read_b64_tr_b16 v[120:121], v10 offset:32768\n"
    "ds_read_b64_tr_b16 v[122:123], v10 offset:34816\n"
    "v_mfma_f32_16x16x32_bf16 v[92:95], v[140:143], v[56:59], v[92:95]\n"
    "ds_read_b64_tr_b16 v[124:125], v11 offset:32768\n"
    "ds_read_b64_tr_b16 v[126:127], v11 offset:34816\n"
    "v_mfma_f32_16x16x32_bf16 v[96:99], v[128:131], v[60:63], v[96:99]\n"
    "buffer_load_dwordx4 v[52:55], v17, s[28:31], s16 offen offset:1024 sc1\n"
    "v_mfma_f32_16x16x32_bf16 v[100:103], v[132:135], v[60:63], v[100:103]\n"
    "v_mfma_f32_16x16x32_bf16 v[104:107], v[136:139], v[60:63], v[104:107]\n"
    "v_mfma_f32_16x16x32_bf16 v[108:111], v[140:143], v[60:63], v[108:111]\n"
    "s_waitcnt vmcnt(9)\n"
    "s_waitcnt lgkmcnt(0)\n"
    "v_mfma_f32_16x16x32_bf16 v[80:83], v[112:115], v[64:67], v[80:83]\n"
    "ds_read_b64_tr_b16 v[128:129], v8 offset:49152\n"
    "ds_read_b64_tr_b16 v[130:131], v8 offset:51200\n"
    "buffer_load_dwordx4 v[56:59], v17, s[28:31], s17 offen offset:0 sc1\n"
    "v_mfma_f32_16x16x32_bf16 v[84:87], v[116:119], v[64:67], v[84:87]\n"
    "ds_read_b64_tr_b16 v[132:133], v9 offset:49152\n"
    "ds_read_b64_tr_b16 v[134:135], v9 offset:51200\n"
    "v_mfma_f32_16x16x32_bf16 v[88:91], v[120:123], v[64:67], v[88:91]\n"
    "ds_read_b64_tr_b16 v[136:137], v10 offset:49152\n"
    "ds_read_b64_tr_b16 v[138:139], v10 offset:51200\n"
    "v_mfma_f32_16x16x32_bf16 v[92:95], v[124:127], v[64:67], v[92:95]\n"
    "ds_read_b64_tr_b16 v[140:141], v11 offset:49152\n"
    "ds_read_b64_tr_b16 v[142:143], v11 offset:51200\n"
    "v_mfma_f32_16x16x32_bf16 v[96:99], v[112:115], v[68:71], v[96:99]\n"
    "buffer_load_dwordx4 v[60:63], v17, s[28:31], s17 offen offset:1024 sc1\n"
    "v_mfma_f32_16x16x32_bf16 v[100:103], v[116:119], v[68:71], v[100:103]\n"
    "v_mfma_f32_16x16x32_bf16 v[104:107], v[120:123], v[68:71], v[104:107]\n"
    "v_mfma_f32_16x16x32_bf16 v[108:111], v[124:127], v[68:71], v[108:111]\n"
    "s_waitcnt vmcnt(9)\n"
    "s_waitcnt lgkmcnt(0)\n"
    "v_mfma_f32_16x16x32_bf16 v[80:83], v[128:131], v[72:75], v[80:83]\n"
    "ds_read_b64_tr_b16 v[112:113], v12 offset:0\n"
    "ds_read_b64_tr_b16 v[114:115], v12 offset:2048\n"
    "buffer_load_dwordx4 v[64:67], v17, s[28:31], s18 offen offset:0 sc1\n"
    "v_mfma_f32_16x16x32_bf16 v[84:87], v[132:135], v[72:75], v[84:87]\n"
    "ds_read_b64_tr_b16 v[116:117], v7 offset:0\n"
    "ds_read_b64_tr_b16 v[118:119], v7 offset:2048\n"
    "v_mfma_f32_16x16x32_bf16 v[88:91], v[136:139], v[72:75], v[88:91]\n"
    "ds_read_b64_tr_b16 v[120:121], v14 offset:0\n"
    "ds_read_b64_tr_b16 v[122:123], v14 offset:2048\n"
    "v_mfma_f32_16x16x32_bf16 v[92:95], v[140:143], v[72:75], v[92:95]\n"
    "ds_read_b64_tr_b16 v[124:125], v15 offset:0\n"
    "ds_read_b64_tr_b16 v[126:127], v15 offset:2048\n"
    "v_mfma_f32_16x16x32_bf16 v[96:99], v[128:131], v[76:79], v[96:99]\n"
    "buffer_load_dwordx4 v[68:71], v17, s[28:31], s18 offen offset:1024 sc1\n"
    "v_mfma_f32_16x16x32_bf16 v[100:103], v[132:135], v[76:79], v[100:103]\n"
    "v_mfma_f32_16x16x32_bf16 v[104:107], v[136:139], v[76:79], v[104:107]\n"
    "v_mfma_f32_16x16x32_bf16 v[108:111], v[140:143], v[76:79], v[108:111]\n"
    "s_waitcnt vmcnt(8)\n"
    "s_waitcnt lgkmcnt(0)\n"
    "v_mfma_f32_16x16x32_bf16 v[80:83], v[112:115], v[32:35], v[80:83]\n"
    "ds_read_b64_tr_b16 v[128:129], v12 offset:16384\n"
    "ds_read_b64_tr_b16 v[130:131], v12 offset:18432\n"
    "buffer_load_dwordx4 v[72:75], v17, s[28:31], s19 offen offset:0 sc1\n"
    "v_mfma_f32_16x16x32_bf16 v[84:87], v[116:119], v[32:35], v[84:87]\n"
    "ds_read_b64_tr_b16 v[132:133], v7 offset:16384\n"
    "ds_read_b64_tr_b16 v[134:135], v7 offset:18432\n"
    "v_mfma_f32_16x16x32_bf16 v[88:91], v[120:123], v[32:35], v[88:91]\n"
    "ds_read_b64_tr_b16 v[136:137], v14 offset:16384\n"
    "ds_read_b64_tr_b16 v[138:139], v14 offset:18432\n"
    "v_mfma_f32_16x16x32_bf16 v[92:95], v[124:127], v[32:35], v[92:95]\n"
    "ds_read_b64_tr_b16 v[140:141], v15 offset:16384\n"
    "ds_read_b64_tr_b16 v[142:143], v15 offset:18432\n"
    "v_mfma_f32_16x16x32_bf16 v[96:99], v[112:115], v[36:39], v[96:99]\n"
    "buffer_load_dwordx4 v[76:79], v17, s[28:31], s19 offen offset:1024 sc1\n"
    "v_mfma_f32_16x16x32_bf16 v[100:103], v[116:119], v[36:39], v[100:103]\n"
    "v_mfma_f32_16x16x32_bf16 v[104:107], v[120:123], v[36:39], v[104:107]\n"
    "v_mfma_f32_16x16x32_bf16 v[108:111], v[124:127], v[36:39], v[108:111]\n"
    "s_waitcnt vmcnt(8)\n"
    "s_waitcnt lgkmcnt(0)\n"
    "v_mfma_f32_16x16x32_bf16 v[80:83], v[128:131], v[40:43], v[80:83]\n"
    "buffer_load_dwordx4 v[32:35], v17, s[28:31], s20 offen offset:0 sc1\n"
    "v_mfma_f32_16x16x32_bf16 v[84:87], v[132:135], v[40:43], v[84:87]\n"
    "v_mfma_f32_16x16x32_bf16 v[88:91], v[136:139], v[40:43], v[88:91]\n"
    "v_mfma_f32_16x16x32_bf16 v[92:95], v[140:143], v[40:43], v[92:95]\n"
    "v_mfma_f32_16x16x32_bf16 v[96:99], v[128:131], v[44:47], v[96:99]\n"
    "buffer_load_dwordx4 v[36:39], v17, s[28:31], s20 offen offset:1024 sc1\n"
    "v_mfma_f32_16x16x32_bf16 v[100:103], v[132:135], v[44:47], v[100:103]\n"
    "v_mfma_f32_16x16x32_bf16 v[104:107], v[136:139], v[44:47], v[104:107]\n"
    "v_mfma_f32_16x16x32_bf16 v[108:111], v[140:143], v[44:47], v[108:111]\n"
    "s_waitcnt vmcnt(8)\n"
    "v_mfma_f32_16x16x32_bf16 v[112:115], v[144:147], v[48:51], 0\n"
    "buffer_load_dwordx4 v[40:43], v17, s[28:31], s21 offen offset:0 sc1\n"
    "v_mfma_f32_16x16x32_bf16 v[116:119], v[148:151], v[48:51], 0\n"
    "v_mfma_f32_16x16x32_bf16 v[120:123], v[152:155], v[48:51], 0\n"
    "v_mfma_f32_16x16x32_bf16 v[124:127], v[156:159], v[48:51], 0\n"
    "v_mfma_f32_16x16x32_bf16 v[128:131], v[144:147], v[52:55], 0\n"
    "buffer_load_dwordx4 v[44:47], v17, s[28:31], s21 offen offset:1024 sc1\n"
    "v_mfma_f32_16x16x32_bf16 v[132:135], v[148:151], v[52:55], 0\n"
    "v_mfma_f32_16x16x32_bf16 v[136:139], v[152:155], v[52:55], 0\n"
    "v_mfma_f32_16x16x32_bf16 v[140:143], v[156:159], v[52:55], 0\n"
    "s_waitcnt vmcnt(8)\n"
    "v_add_u32 v2, 0x10000, v2\n"
    "v_add_u32 v3, 0x10000, v3\n"
    "v_mfma_f32_16x16x32_bf16 v[112:115], v[160:163], v[56:59], v[112:115]\n"
    "ds_write_b128 v2, v[80:83] offset:32768\n"
    "ds_write_b128 v3, v[84:87] offset:32768\n"
    "buffer_load_dwordx4 v[48:51], v17, s[28:31], s22 offen offset:0 sc1\n"
    "v_mfma_f32_16x16x32_bf16 v[116:119], v[164:167], v[56:59], v[116:119]\n"
    "ds_write_b128 v2, v[88:91] offset:32896\n"
    "ds_write_b128 v3, v[92:95] offset:32896\n"
    "v_mfma_f32_16x16x32_bf16 v[120:123], v[168:171], v[56:59], v[120:123]\n"
    "v_mfma_f32_16x16x32_bf16 v[124:127], v[172:175], v[56:59], v[124:127]\n"
    "v_mfma_f32_16x16x32_bf16 v[128:131], v[160:163], v[60:63], v[128:131]\n"
    "buffer_load_dwordx4 v[52:55], v17, s[28:31], s22 offen offset:1024 sc1\n"
    "v_mfma_f32_16x16x32_bf16 v[132:135], v[164:167], v[60:63], v[132:135]\n"
    "v_mfma_f32_16x16x32_bf16 v[136:139], v[168:171], v[60:63], v[136:139]\n"
    "v_mfma_f32_16x16x32_bf16 v[140:143], v[172:175], v[60:63], v[140:143]\n"
    "s_waitcnt vmcnt(8)\n"
    "v_mfma_f32_16x16x32_bf16 v[112:115], v[176:179], v[64:67], v[112:115]\n"
    "ds_write_b128 v2, v[96:99] offset:49152\n"
    "ds_write_b128 v3, v[100:103] offset:49152\n"
    "buffer_load_dwordx4 v[56:59], v17, s[28:31], s23 offen offset:0 sc1\n"
    "v_mfma_f32_16x16x32_bf16 v[116:119], v[180:183], v[64:67], v[116:119]\n"
    "ds_write_b128 v2, v[104:107] offset:49280\n"
    "ds_write_b128 v3, v[108:111] offset:49280\n"
    "v_mfma_f32_16x16x32_bf16 v[120:123], v[184:187], v[64:67], v[120:123]\n"
    "v_mfma_f32_16x16x32_bf16 v[124:127], v[188:191], v[64:67], v[124:127]\n"
    "v_mfma_f32_16x16x32_bf16 v[128:131], v[176:179], v[68:71], v[128:131]\n"
    "buffer_load_dwordx4 v[60:63], v17, s[28:31], s23 offen offset:1024 sc1\n"
    "v_mfma_f32_16x16x32_bf16 v[132:135], v[180:183], v[68:71], v[132:135]\n"
    "v_mfma_f32_16x16x32_bf16 v[136:139], v[184:187], v[68:71], v[136:139]\n"
    "v_mfma_f32_16x16x32_bf16 v[140:143], v[188:191], v[68:71], v[140:143]\n"
    "s_waitcnt vmcnt(8)\n"
    "v_mfma_f32_16x16x32_bf16 v[112:115], v[192:195], v[72:75], v[112:115]\n"
    "buffer_load_dwordx4 v[64:67], v17, s[28:31], s24 offen offset:0 sc1\n"
    "v_mfma_f32_16x16x32_bf16 v[116:119], v[196:199], v[72:75], v[116:119]\n"
    "v_mfma_f32_16x16x32_bf16 v[120:123], v[200:203], v[72:75], v[120:123]\n"
    "v_mfma_f32_16x16x32_bf16 v[124:127], v[204:207], v[72:75], v[124:127]\n"
    "v_mfma_f32_16x16x32_bf16 v[128:131], v[192:195], v[76:79], v[128:131]\n"
    "buffer_load_dwordx4 v[68:71], v17, s[28:31], s24 offen offset:1024 sc1\n"
    "v_mfma_f32_16x16x32_bf16 v[132:135], v[196:199], v[76:79], v[132:135]\n"
    "v_mfma_f32_16x16x32_bf16 v[136:139], v[200:203], v[76:79], v[136:139]\n"
    "v_mfma_f32_16x16x32_bf16 v[140:143], v[204:207], v[76:79], v[140:143]\n"
    "s_waitcnt vmcnt(8)\n"
    "s_waitcnt lgkmcnt(0)\n"
    "s_barrier\n"
    "v_subrev_u32 v2, 0x10000, v2\n"
    "v_subrev_u32 v3, 0x10000, v3\n"
    "v_mfma_f32_16x16x32_bf16 v[112:115], v[208:211], v[32:35], v[112:115]\n"
    "buffer_load_dwordx4 v[72:75], v17, s[28:31], s25 offen offset:0 sc1\n"
    "v_mfma_f32_16x16x32_bf16 v[116:119], v[212:215], v[32:35], v[116:119]\n"
    "v_mfma_f32_16x16x32_bf16 v[120:123], v[216:219], v[32:35], v[120:123]\n"
    "v_mfma_f32_16x16x32_bf16 v[124:127], v[220:223], v[32:35], v[124:127]\n"
    "v_mfma_f32_16x16x32_bf16 v[128:131], v[208:211], v[36:39], v[128:131]\n"
    "buffer_load_dwordx4 v[76:79], v17, s[28:31], s25 offen offset:1024 sc1\n"
    "v_mfma_f32_16x16x32_bf16 v[132:135], v[212:215], v[36:39], v[132:135]\n"
    "v_mfma_f32_16x16x32_bf16 v[136:139], v[216:219], v[36:39], v[136:139]\n"
    "v_mfma_f32_16x16x32_bf16 v[140:143], v[220:223], v[36:39], v[140:143]\n"
    "s_waitcnt vmcnt(8)\n"
    "v_mfma_f32_16x16x32_bf16 v[112:115], v[224:227], v[40:43], v[112:115]\n"
    "buffer_load_dwordx4 v[32:35], v17, s[28:31], s26 offen offset:0 sc1\n"
    "v_mfma_f32_16x16x32_bf16 v[116:119], v[228:231], v[40:43], v[116:119]\n"
    "v_mfma_f32_16x16x32_bf16 v[120:123], v[232:235], v[40:43], v[120:123]\n"
    "v_mfma_f32_16x16x32_bf16 v[124:127], v[236:239], v[40:43], v[124:127]\n"
    "v_mfma_f32_16x16x32_bf16 v[128:131], v[224:227], v[44:47], v[128:131]\n"
    "buffer_load_dwordx4 v[36:39], v17, s[28:31], s26 offen offset:1024 sc1\n"
    "v_mfma_f32_16x16x32_bf16 v[132:135], v[228:231], v[44:47], v[132:135]\n"
    "v_mfma_f32_16x16x32_bf16 v[136:139], v[232:235], v[44:47], v[136:139]\n"
    "v_mfma_f32_16x16x32_bf16 v[140:143], v[236:239], v[44:47], v[140:143]\n"
    "s_waitcnt vmcnt(8)\n"
    "v_mfma_f32_16x16x32_bf16 v[112:115], v[240:243], v[48:51], v[112:115]\n"
    "ds_read_b64_tr_b16 v[96:97], v20 offset:0\n"
    "ds_read_b64_tr_b16 v[98:99], v20 offset:2048\n"
    "buffer_load_dwordx4 v[40:43], v17, s[28:31], s27 offen offset:0 sc1\n"
    "v_mfma_f32_16x16x32_bf16 v[116:119], v[244:247], v[48:51], v[116:119]\n"
    "ds_read_b64_tr_b16 v[100:101], v21 offset:0\n"
    "ds_read_b64_tr_b16 v[102:103], v21 offset:2048\n"
    "v_mfma_f32_16x16x32_bf16 v[120:123], v[248:251], v[48:51], v[120:123]\n"
    "ds_read_b64_tr_b16 v[104:105], v22 offset:0\n"
    "ds_read_b64_tr_b16 v[106:107], v22 offset:2048\n"
    "v_mfma_f32_16x16x32_bf16 v[124:127], v[252:255], v[48:51], v[124:127]\n"
    "ds_read_b64_tr_b16 v[108:109], v23 offset:0\n"
    "ds_read_b64_tr_b16 v[110:111], v23 offset:2048\n"
    "v_mfma_f32_16x16x32_bf16 v[128:131], v[240:243], v[52:55], v[128:131]\n"
    "buffer_load_dwordx4 v[44:47], v17, s[28:31], s27 offen offset:1024 sc1\n"
    "v_mfma_f32_16x16x32_bf16 v[132:135], v[244:247], v[52:55], v[132:135]\n"
    "v_mfma_f32_16x16x32_bf16 v[136:139], v[248:251], v[52:55], v[136:139]\n"
    "v_mfma_f32_16x16x32_bf16 v[140:143], v[252:255], v[52:55], v[140:143]\n"
    "s_waitcnt vmcnt(8)\n"
    "s_waitcnt lgkmcnt(0)\n"
    "v_mfma_f32_16x16x32_bf16 v[112:115], v[96:99], v[56:59], v[112:115]\n"
    "ds_read_b64_tr_b16 v[80:81], v8 offset:0\n"
    "ds_read_b64_tr_b16 v[82:83], v8 offset:2048\n"
    "buffer_load_dwordx4 v[48:51], v17, s[28:31], s52 offen offset:0 sc1\n"
    "v_mfma_f32_16x16x32_bf16 v[116:119], v[100:103], v[56:59], v[116:119]\n"
    "ds_read_b64_tr_b16 v[84:85], v9 offset:0\n"
    "ds_read_b64_tr_b16 v[86:87], v9 offset:2048\n"
    "v_mfma_f32_16x16x32_bf16 v[120:123], v[104:107], v[56:59], v[120:123]\n"
    "ds_read_b64_tr_b16 v[88:89], v10 offset:0\n"
    "ds_read_b64_tr_b16 v[90:91], v10 offset:2048\n"
    "v_mfma_f32_16x16x32_bf16 v[124:127], v[108:111], v[56:59], v[124:127]\n"
    "ds_read_b64_tr_b16 v[92:93], v11 offset:0\n"
    "ds_read_b64_tr_b16 v[94:95], v11 offset:2048\n"
    "v_mfma_f32_16x16x32_bf16 v[128:131], v[96:99], v[60:63], v[128:131]\n"
    "buffer_load_dwordx4 v[52:55], v17, s[28:31], s52 offen offset:1024 sc1\n"
    "v_mfma_f32_16x16x32_bf16 v[132:135], v[100:103], v[60:63], v[132:135]\n"
    "v_mfma_f32_16x16x32_bf16 v[136:139], v[104:107], v[60:63], v[136:139]\n"
    "v_mfma_f32_16x16x32_bf16 v[140:143], v[108:111], v[60:63], v[140:143]\n"
    "s_waitcnt vmcnt(8)\n"
    "s_waitcnt lgkmcnt(0)\n"
    "v_mfma_f32_16x16x32_bf16 v[112:115], v[80:83], v[64:67], v[112:115]\n"
    "ds_read_b64_tr_b16 v[96:97], v8 offset:16384\n"
    "ds_read_b64_tr_b16 v[98:99], v8 offset:18432\n"
    "buffer_load_dwordx4 v[56:59], v17, s[28:31], s53 offen offset:0 sc1\n"
    "v_mfma_f32_16x16x32_bf16 v[116:119], v[84:87], v[64:67], v[116:119]\n"
    "ds_read_b64_tr_b16 v[100:101], v9 offset:16384\n"
    "ds_read_b64_tr_b16 v[102:103], v9 offset:18432\n"
    "v_mfma_f32_16x16x32_bf16 v[120:123], v[88:91], v[64:67], v[120:123]\n"
    "ds_read_b64_tr_b16 v[104:105], v10 offset:16384\n"
    "ds_read_b64_tr_b16 v[106:107], v10 offset:18432\n"
    "v_mfma_f32_16x16x32_bf16 v[124:127], v[92:95], v[64:67], v[124:127]\n"
    "ds_read_b64_tr_b16 v[108:109], v11 offset:16384\n"
    "ds_read_b64_tr_b16 v[110:111], v11 offset:18432\n"
    "v_mfma_f32_16x16x32_bf16 v[128:131], v[80:83], v[68:71], v[128:131]\n"
    "buffer_load_dwordx4 v[60:63], v17, s[28:31], s53 offen offset:1024 sc1\n"
    "v_mfma_f32_16x16x32_bf16 v[132:135], v[84:87], v[68:71], v[132:135]\n"
    "v_mfma_f32_16x16x32_bf16 v[136:139], v[88:91], v[68:71], v[136:139]\n"
    "v_mfma_f32_16x16x32_bf16 v[140:143], v[92:95], v[68:71], v[140:143]\n"
    "s_waitcnt vmcnt(8)\n"
    "s_waitcnt lgkmcnt(0)\n"
    "v_mfma_f32_16x16x32_bf16 v[112:115], v[96:99], v[72:75], v[112:115]\n"
    "ds_read_b64_tr_b16 v[80:81], v8 offset:32768\n"
    "ds_read_b64_tr_b16 v[82:83], v8 offset:34816\n"
    "v_mfma_f32_16x16x32_bf16 v[116:119], v[100:103], v[72:75], v[116:119]\n"
    "ds_read_b64_tr_b16 v[84:85], v9 offset:32768\n"
    "ds_read_b64_tr_b16 v[86:87], v9 offset:34816\n"
    "v_mfma_f32_16x16x32_bf16 v[120:123], v[104:107], v[72:75], v[120:123]\n"
    "ds_read_b64_tr_b16 v[88:89], v10 offset:32768\n"
    "ds_read_b64_tr_b16 v[90:91], v10 offset:34816\n"
    "v_mfma_f32_16x16x32_bf16 v[124:127], v[108:111], v[72:75], v[124:127]\n"
    "ds_read_b64_tr_b16 v[92:93], v11 offset:32768\n"
    "ds_read_b64_tr_b16 v[94:95], v11 offset:34816\n"
    "v_mfma_f32_16x16x32_bf16 v[128:131], v[96:99], v[76:79], v[128:131]\n"
    "v_mfma_f32_16x16x32_bf16 v[132:135], v[100:103], v[76:79], v[132:135]\n"
    "v_mfma_f32_16x16x32_bf16 v[136:139], v[104:107], v[76:79], v[136:139]\n"
    "v_mfma_f32_16x16x32_bf16 v[140:143], v[108:111], v[76:79], v[140:143]\n"
    "s_waitcnt vmcnt(6)\n"
    "s_waitcnt lgkmcnt(0)\n"
    "v_mfma_f32_16x16x32_bf16 v[112:115], v[80:83], v[32:35], v[112:115]\n"
    "ds_read_b64_tr_b16 v[96:97], v8 offset:49152\n"
    "ds_read_b64_tr_b16 v[98:99], v8 offset:51200\n"
    "v_mfma_f32_16x16x32_bf16 v[116:119], v[84:87], v[32:35], v[116:119]\n"
    "ds_read_b64_tr_b16 v[100:101], v9 offset:49152\n"
    "ds_read_b64_tr_b16 v[102:103], v9 offset:51200\n"
    "v_mfma_f32_16x16x32_bf16 v[120:123], v[88:91], v[32:35], v[120:123]\n"
    "ds_read_b64_tr_b16 v[104:105], v10 offset:49152\n"
    "ds_read_b64_tr_b16 v[106:107], v10 offset:51200\n"
    "v_mfma_f32_16x16x32_bf16 v[124:127], v[92:95], v[32:35], v[124:127]\n"
    "ds_read_b64_tr_b16 v[108:109], v11 offset:49152\n"
    "ds_read_b64_tr_b16 v[110:111], v11 offset:51200\n"
    "v_mfma_f32_16x16x32_bf16 v[128:131], v[80:83], v[36:39], v[128:131]\n"
    "v_mfma_f32_16x16x32_bf16 v[132:135], v[84:87], v[36:39], v[132:135]\n"
    "v_mfma_f32_16x16x32_bf16 v[136:139], v[88:91], v[36:39], v[136:139]\n"
    "v_mfma_f32_16x16x32_bf16 v[140:143], v[92:95], v[36:39], v[140:143]\n"
    "s_waitcnt vmcnt(4)\n"
    "s_waitcnt lgkmcnt(0)\n"
    "v_mfma_f32_16x16x32_bf16 v[112:115], v[96:99], v[40:43], v[112:115]\n"
    "ds_read_b64_tr_b16 v[80:81], v12 offset:0\n"
    "ds_read_b64_tr_b16 v[82:83], v12 offset:2048\n"
    "v_mfma_f32_16x16x32_bf16 v[116:119], v[100:103], v[40:43], v[116:119]\n"
    "ds_read_b64_tr_b16 v[84:85], v7 offset:0\n"
    "ds_read_b64_tr_b16 v[86:87], v7 offset:2048\n"
    "v_mfma_f32_16x16x32_bf16 v[120:123], v[104:107], v[40:43], v[120:123]\n"
    "ds_read_b64_tr_b16 v[88:89], v14 offset:0\n"
    "ds_read_b64_tr_b16 v[90:91], v14 offset:2048\n"
    "v_mfma_f32_16x16x32_bf16 v[124:127], v[108:111], v[40:43], v[124:127]\n"
    "ds_read_b64_tr_b16 v[92:93], v15 offset:0\n"
    "ds_read_b64_tr_b16 v[94:95], v15 offset:2048\n"
    "v_mfma_f32_16x16x32_bf16 v[128:131], v[96:99], v[44:47], v[128:131]\n"
    "v_mfma_f32_16x16x32_bf16 v[132:135], v[100:103], v[44:47], v[132:135]\n"
    "v_mfma_f32_16x16x32_bf16 v[136:139], v[104:107], v[44:47], v[136:139]\n"
    "v_mfma_f32_16x16x32_bf16 v[140:143], v[108:111], v[44:47], v[140:143]\n"
    "s_waitcnt vmcnt(2)\n"
    "s_waitcnt lgkmcnt(0)\n"
    "v_mfma_f32_16x16x32_bf16 v[112:115], v[80:83], v[48:51], v[112:115]\n"
    "ds_read_b64_tr_b16 v[96:97], v12 offset:16384\n"
    "ds_read_b64_tr_b16 v[98:99], v12 offset:18432\n"
    "v_mfma_f32_16x16x32_bf16 v[116:119], v[84:87], v[48:51], v[116:119]\n"
    "ds_read_b64_tr_b16 v[100:101], v7 offset:16384\n"
    "ds_read_b64_tr_b16 v[102:103], v7 offset:18432\n"
    "v_mfma_f32_16x16x32_bf16 v[120:123], v[88:91], v[48:51], v[120:123]\n"
    "ds_read_b64_tr_b16 v[104:105], v14 offset:16384\n"
    "ds_read_b64_tr_b16 v[106:107], v14 offset:18432\n"
    "v_mfma_f32_16x16x32_bf16 v[124:127], v[92:95], v[48:51], v[124:127]\n"
    "ds_read_b64_tr_b16 v[108:109], v15 offset:16384\n"
    "ds_read_b64_tr_b16 v[110:111], v15 offset:18432\n"
    "v_mfma_f32_16x16x32_bf16 v[128:131], v[80:83], v[52:55], v[128:131]\n"
    "v_mfma_f32_16x16x32_bf16 v[132:135], v[84:87], v[52:55], v[132:135]\n"
    "v_mfma_f32_16x16x32_bf16 v[136:139], v[88:91], v[52:55], v[136:139]\n"
    "v_mfma_f32_16x16x32_bf16 v[140:143], v[92:95], v[52:55], v[140:143]\n"
    "s_waitcnt vmcnt(0)\n"
    "s_waitcnt lgkmcnt(0)\n"
    "v_mfma_f32_16x16x32_bf16 v[112:115], v[96:99], v[56:59], v[112:115]\n"
    "v_mfma_f32_16x16x32_bf16 v[116:119], v[100:103], v[56:59], v[116:119]\n"
    "v_mfma_f32_16x16x32_bf16 v[120:123], v[104:107], v[56:59], v[120:123]\n"
    "v_mfma_f32_16x16x32_bf16 v[124:127], v[108:111], v[56:59], v[124:127]\n"
    "v_mfma_f32_16x16x32_bf16 v[128:131], v[96:99], v[60:63], v[128:131]\n"
    "v_mfma_f32_16x16x32_bf16 v[132:135], v[100:103], v[60:63], v[132:135]\n"
    "v_mfma_f32_16x16x32_bf16 v[136:139], v[104:107], v[60:63], v[136:139]\n"
    "v_mfma_f32_16x16x32_bf16 v[140:143], v[108:111], v[60:63], v[140:143]\n"
    "s_nop 0\n"
    "ds_write_b128 v2, v[112:115] offset:0\n"
    "ds_write_b128 v3, v[116:119] offset:0\n"
    "ds_write_b128 v2, v[120:123] offset:128\n"
    "ds_write_b128 v3, v[124:127] offset:128\n"
    "ds_write_b128 v2, v[128:131] offset:16384\n"
    "ds_write_b128 v3, v[132:135] offset:16384\n"
    "ds_write_b128 v2, v[136:139] offset:16512\n"
    "ds_write_b128 v3, v[140:143] offset:16512\n"
    "s_waitcnt lgkmcnt(0)\n"
    "s_barrier\n"
    "s_endpgm\n"
    ".Lh3_B_spare:\n"
    "s_memrealtime s[60:61]\n"
    "s_mov_b32 s66, s13\n"
    ".Lh3_SP_stats:\n"
    "s_and_b32 s37, s4, 1\n"
    "s_lshl1_add_u32 s37, s9, s37\n"
    "s_lshl_b32 s58, s37, 16\n"
    "s_add_u32 s58, s58, 4292608\n"
    "s_mov_b32 s59, s63\n"
    "s_lshl_b32 s37, s37, 8\n"
    "s_add_u32 s37, s37, 32768\n"
    "s_add_u32 s36, s37, 8192\n"
    "v_and_b32 v24, 15, v1\n"
    "v_lshlrev_b32 v24, 3, v24\n"
    "s_lshl_b32 s66, s66, 9\n"
    "s_add_u32 s66, s66, 147456\n"
    "v_add_u32 v24, s66, v24\n"
    "v_and_b32 v25, 3, v1\n"
    "v_lshrrev_b32 v26, 2, v1\n"
    "v_lshl_add_u32 v25, v25, 4, v26\n"
    "v_lshlrev_b32 v25, 3, v25\n"
    "v_add_u32 v25, 147456, v25\n"
    "v_lshlrev_b32 v26, 2, v1\n"
    "s_waitcnt vmcnt(0) lgkmcnt(0)\n"
    "s_barrier\n"
    "s_add_u32 m0, s63, 0\n"
    "s_add_u32 s66, s59, 0\n"
    "s_and_b32 s66, s66, 0xffff\n"
    "s_add_u32 s66, s66, s58\n"
    "s_waitcnt vmcnt(7)\n"
    "buffer_load_dwordx4 v5, s[28:31], s66 offen offset:0 sc1 lds\n"
    "s_waitcnt vmcnt(7)\n"
    "buffer_load_dwordx4 v5, s[28:31], s66 offen offset:1024 sc1 lds\n"
    "s_waitcnt vmcnt(7)\n"
    "buffer_load_dwordx4 v5, s[28:31], s66 offen offset:2048 sc1 lds\n"
    "s_waitcnt vmcnt(7)\n"
    "buffer_load_dwordx4 v5, s[28:31], s66 offen offset:3072 sc1 lds\n"
    "s_add_u32 m0, s63, 16384\n"
    "s_add_u32 s66, s59, 16384\n"
    "s_and_b32 s66, s66, 0xffff\n"
    "s_add_u32 s66, s66, s58\n"
    "s_waitcnt vmcnt(7)\n"
    "buffer_load_dwordx4 v5, s[28:31], s66 offen offset:0 sc1 lds\n"
    "s_waitcnt vmcnt(7)\n"
    "buffer_load_dwordx4 v5, s[28:31], s66 offen offset:1024 sc1 lds\n"
    "s_waitcnt vmcnt(7)\n"
    "buffer_load_dwordx4 v5, s[28:31], s66 offen offset:2048 sc1 lds\n"
    "s_waitcnt vmcnt(7)\n"
    "buffer_load_dwordx4 v5, s[28:31], s66 offen offset:3072 sc1 lds\n"
    "s_waitcnt vmcnt(4)\n"
    "ds_read_b64_tr_b16 v[80:81], v8 offset:0\n"
    "ds_read_b64_tr_b16 v[82:83], v8 offset:2048\n"
    "ds_read_b64_tr_b16 v[84:85], v9 offset:0\n"
    "ds_read_b64_tr_b16 v[86:87], v9 offset:2048\n"
    "ds_read_b64_tr_b16 v[88:89], v10 offset:0\n"
    "ds_read_b64_tr_b16 v[90:91], v10 offset:2048\n"
    "ds_read_b64_tr_b16 v[92:93], v11 offset:0\n"
    "ds_read_b64_tr_b16 v[94:95], v11 offset:2048\n"
    "s_waitcnt lgkmcnt(0)\n"
    "v_dot2_f32_bf16 v104, v80, v19, 0\n"
    "v_dot2_f32_bf16 v106, v84, v19, 0\n"
    "v_dot2_f32_bf16 v108, v88, v19, 0\n"
    "v_dot2_f32_bf16 v110, v92, v19, 0\n"
    "v_dot2_f32_bf16 v104, v81, v19, v104\n"
    "v_dot2_f32_bf16 v106, v85, v19, v106\n"
    "v_dot2_f32_bf16 v108, v89, v19, v108\n"
    "v_dot2_f32_bf16 v110, v93, v19, v110\n"
    "v_dot2_f32_bf16 v104, v82, v19, v104\n"
    "v_dot2_f32_bf16 v106, v86, v19, v106\n"
    "v_dot2_f32_bf16 v108, v90, v19, v108\n"
    "v_dot2_f32_bf16 v110, v94, v19, v110\n"
    "v_dot2_f32_bf16 v104, v83, v19, v104\n"
    "v_dot2_f32_bf16 v106, v87, v19, v106\n"
    "v_dot2_f32_bf16 v108, v91, v19, v108\n"
    "v_dot2_f32_bf16 v110, v95, v19, v110\n"
    "s_nop 1\n"
    "v_cmp_u_f32 vcc, v104, v106\n"
    "v_cmp_u_f32 s[66:67], v108, v110\n"
    "s_or_b64 vcc, vcc, s[66:67]\n"
    "s_cbranch_vccnz .Lh3_slow_43\n"
    ".Lh3_back_44:\n"
    "v_dot2_f32_bf16 v97, v80, v80, 0\n"
    "v_dot2_f32_bf16 v99, v84, v84, 0\n"
    "v_dot2_f32_bf16 v101, v88, v88, 0\n"
    "v_dot2_f32_bf16 v103, v92, v92, 0\n"
    "v_dot2_f32_bf16 v97, v81, v81, v97\n"
    "v_dot2_f32_bf16 v99, v85, v85, v99\n"
    "v_dot2_f32_bf16 v101, v89, v89, v101\n"
    "v_dot2_f32_bf16 v103, v93, v93, v103\n"
    "v_dot2_f32_bf16 v97, v82, v82, v97\n"
    "v_dot2_f32_bf16 v99, v86, v86, v99\n"
    "v_dot2_f32_bf16 v101, v90, v90, v101\n"
    "v_dot2_f32_bf16 v103, v94, v94, v103\n"
    "v_dot2_f32_bf16 v97, v83, v83, v97\n"
    "v_dot2_f32_bf16 v99, v87, v87, v99\n"
    "v_dot2_f32_bf16 v101, v91, v91, v101\n"
    "v_dot2_f32_bf16 v103, v95, v95, v103\n"
    "s_waitcnt vmcnt(0)\n"
    "ds_read_b64_tr_b16 v[80:81], v8 offset:16384\n"
    "ds_read_b64_tr_b16 v[82:83], v8 offset:18432\n"
    "ds_read_b64_tr_b16 v[84:85], v9 offset:16384\n"
    "ds_read_b64_tr_b16 v[86:87], v9 offset:18432\n"
    "ds_read_b64_tr_b16 v[88:89], v10 offset:16384\n"
    "ds_read_b64_tr_b16 v[90:91], v10 offset:18432\n"
    "ds_read_b64_tr_b16 v[92:93], v11 offset:16384\n"
    "ds_read_b64_tr_b16 v[94:95], v11 offset:18432\n"
    "s_waitcnt lgkmcnt(0)\n"
    "v_dot2_f32_bf16 v96, v80, v19, v104\n"
    "v_dot2_f32_bf16 v98, v84, v19, v106\n"
    "v_dot2_f32_bf16 v100, v88, v19, v108\n"
    "v_dot2_f32_bf16 v102, v92, v19, v110\n"
    "v_dot2_f32_bf16 v96, v81, v19, v96\n"
    "v_dot2_f32_bf16 v98, v85, v19, v98\n"
    "v_dot2_f32_bf16 v100, v89, v19, v100\n"
    "v_dot2_f32_bf16 v102, v93, v19, v102\n"
    "v_dot2_f32_bf16 v96, v82, v19, v96\n"
    "v_dot2_f32_bf16 v98, v86, v19, v98\n"
    "v_dot2_f32_bf16 v100, v90, v19, v100\n"
    "v_dot2_f32_bf16 v102, v94, v19, v102\n"
    "v_dot2_f32_bf16 v96, v83, v19, v96\n"
    "v_dot2_f32_bf16 v98, v87, v19, v98\n"
    "v_dot2_f32_bf16 v100, v91, v19, v100\n"
    "v_dot2_f32_bf16 v102, v95, v19, v102\n"
    "s_nop 1\n"
    "v_cmp_u_f32 vcc, v96, v98\n"
    "v_cmp_u_f32 s[66:67], v100, v102\n"
    "s_or_b64 vcc, vcc, s[66:67]\n"
    "s_cbranch_vccnz .Lh3_slow_45\n"
    ".Lh3_back_46:\n"
    "v_dot2_f32_bf16 v97, v80, v80, v97\n"
    "v_dot2_f32_bf16 v99, v84, v84, v99\n"
    "v_dot2_f32_bf16 v101, v88, v88, v101\n"
    "v_dot2_f32_bf16 v103, v92, v92, v103\n"
    "v_dot2_f32_bf16 v97, v81, v81, v97\n"
    "v_dot2_f32_bf16 v99, v85, v85, v99\n"
    "v_dot2_f32_bf16 v101, v89, v89, v101\n"
    "v_dot2_f32_bf16 v103, v93, v93, v103\n"
    "v_dot2_f32_bf16 v97, v82, v82, v97\n"
    "v_dot2_f32_bf16 v99, v86, v86, v99\n"
    "v_dot2_f32_bf16 v101, v90, v90, v101\n"
    "v_dot2_f32_bf16 v103, v94, v94, v103\n"
    "v_dot2_f32_bf16 v97, v83, v83, v97\n"
    "v_dot2_f32_bf16 v99, v87, v87, v99\n"
    "v_dot2_f32_bf16 v101, v91, v91, v101\n"
    "v_dot2_f32_bf16 v103, v95, v95, v103\n"
    "v_mov_b32 v104, v96\n"
    "v_mov_b32 v105, v97\n"
    "v_mov_b32 v106, v98\n"
    "v_mov_b32 v107, v99\n"
    "v_mov_b32 v108, v100\n"
    "v_mov_b32 v109, v101\n"
    "v_mov_b32 v110, v102\n"
    "v_mov_b32 v111, v103\n"
    "v_permlane16_swap_b32 v96, v104\n"
    "v_permlane16_swap_b32 v97, v105\n"
    "v_permlane16_swap_b32 v98, v106\n"
    "v_permlane16_swap_b32 v99, v107\n"
    "v_permlane16_swap_b32 v100, v108\n"
    "v_permlane16_swap_b32 v101, v109\n"
    "v_permlane16_swap_b32 v102, v110\n"
    "v_permlane16_swap_b32 v103, v111\n"
    "v_add_f32 v96, v96, v104\n"
    "v_add_f32 v97, v97, v105\n"
    "v_add_f32 v98, v98, v106\n"
    "v_add_f32 v99, v99, v107\n"
    "v_add_f32 v100, v100, v108\n"
    "v_add_f32 v101, v101, v109\n"
    "v_add_f32 v102, v102, v110\n"
    "v_add_f32 v103, v103, v111\n"
    "v_mov_b32 v104, v96\n"
    "v_mov_b32 v105, v97\n"
    "v_mov_b32 v106, v98\n"
    "v_mov_b32 v107, v99\n"
    "v_mov_b32 v108, v100\n"
    "v_mov_b32 v109, v101\n"
    "v_mov_b32 v110, v102\n"
    "v_mov_b32 v111, v103\n"
    "v_permlane32_swap_b32 v96, v104\n"
    "v_permlane32_swap_b32 v97, v105\n"
    "v_permlane32_swap_b32 v98, v106\n"
    "v_permlane32_swap_b32 v99, v107\n"
    "v_permlane32_swap_b32 v100, v108\n"
    "v_permlane32_swap_b32 v101, v109\n"
    "v_permlane32_swap_b32 v102, v110\n"
    "v_permlane32_swap_b32 v103, v111\n"
    "v_add_f32 v96, v96, v104\n"
    "v_add_f32 v97, v97, v105\n"
    "v_add_f32 v98, v98, v106\n"
    "v_add_f32 v99, v99, v107\n"
    "v_add_f32 v100, v100, v108\n"
    "v_add_f32 v101, v101, v109\n"
    "v_add_f32 v102, v102, v110\n"
    "v_add_f32 v103, v103, v111\n"
    "ds_write_b64 v24, v[96:97] offset:0\n"
    "ds_write_b64 v24, v[98:99] offset:128\n"
    "ds_write_b64 v24, v[100:101] offset:256\n"
    "ds_write_b64 v24, v[102:103] offset:384\n"
    "s_waitcnt lgkmcnt(0)\n"
    "s_barrier\n"
    "s_cmp_ge_u32 s63, 32768\n"
    "s_cbranch_scc1 .Lh3_A_spare2\n"
    "s_cmp_lg_u32 s13, 0\n"
    "s_cbranch_scc1 .Lh3_B_sp_nost\n"
    "ds_read2st64_b64 v[112:115], v25 offset0:0 offset1:1\n"
    "ds_read2st64_b64 v[116:119], v25 offset0:2 offset1:3\n"
    "ds_read2st64_b64 v[120:123], v25 offset0:4 offset1:5\n"
    "ds_read2st64_b64 v[124:127], v25 offset0:6 offset1:7\n"
    "s_waitcnt lgkmcnt(0)\n"
    "v_pk_add_f32 v[112:113], v[112:113], v[114:115]\n"
    "v_pk_add_f32 v[116:117], v[116:117], v[118:119]\n"
    "v_pk_add_f32 v[120:121], v[120:121], v[122:123]\n"
    "v_pk_add_f32 v[124:125], v[124:125], v[126:127]\n"
    "v_pk_add_f32 v[112:113], v[112:113], v[116:117]\n"
    "v_pk_add_f32 v[120:121], v[120:121], v[124:125]\n"
    "v_pk_add_f32 v[112:113], v[112:113], v[120:121]\n"
    "v_mul_f32 v112, 0x3b000000, v112\n"
    "v_mul_f32 v113, 0x3b000000, v113\n"
    "buffer_store_dword v112, v26, s[28:31], s37 offen sc1\n"
    "v_fma_f32 v113, -v112, v112, v113\n"
    "v_max_f32 v113, 0, v113\n"
    "v_add_f32 v113, 0x3727c5ac, v113\n"
    "v_rsq_f32 v113, v113\n"
    "s_nop 0\n"
    "buffer_store_dword v113, v26, s[28:31], s36 offen sc1\n"
    ".Lh3_B_sp_nost:\n"
    "s_add_u32 m0, s63, 131072\n"
    "s_lshl4_add_u32 s62, s23, s54\n"
    "s_waitcnt vmcnt(7)\n"
    "buffer_load_dwordx4 v13, s[44:47], s62 offen lds\n"
    "s_addk_i32 m0, 0x400\n"
    "s_addk_i32 s62, 8192\n"
    "s_waitcnt vmcnt(7)\n"
    "buffer_load_dwordx4 v13, s[44:47], s62 offen lds\n"
    "s_addk_i32 m0, 0x400\n"
    "s_addk_i32 s62, 8192\n"
    "s_waitcnt vmcnt(7)\n"
    "buffer_load_dwordx4 v13, s[44:47], s62 offen lds\n"
    "s_addk_i32 m0, 0x400\n"
    "s_addk_i32 s62, 8192\n"
    "s_waitcnt vmcnt(7)\n"
    "buffer_load_dwordx4 v13, s[44:47], s62 offen lds\n"
    "s_addk_i32 m0, 0x400\n"
    "s_addk_i32 s62, 8192\n"
    "s_add_u32 m0, s63, 0\n"
    "s_lshl4_add_u32 s62, s24, s54\n"
    "s_waitcnt vmcnt(7)\n"
    "buffer_load_dwordx4 v13, s[44:47], s62 offen lds\n"
    "s_addk_i32 m0, 0x400\n"
    "s_addk_i32 s62, 8192\n"
    "s_waitcnt vmcnt(7)\n"
    "buffer_load_dwordx4 v13, s[44:47], s62 offen lds\n"
    "s_addk_i32 m0, 0x400\n"
    "s_addk_i32 s62, 8192\n"
    "s_waitcnt vmcnt(7)\n"
    "buffer_load_dwordx4 v13, s[44:47], s62 offen lds\n"
    "s_addk_i32 m0, 0x400\n"
    "s_addk_i32 s62, 8192\n"
    "s_waitcnt vmcnt(7)\n"
    "buffer_load_dwordx4 v13, s[44:47], s62 offen lds\n"
    "s_addk_i32 m0, 0x400\n"
    "s_addk_i32 s62, 8192\n"
    "s_add_u32 m0, s63, 16384\n"
    "s_lshl4_add_u32 s62, s25, s54\n"
    "s_waitcnt vmcnt(7)\n"
    "buffer_load_dwordx4 v13, s[44:47], s62 offen lds\n"
    "s_addk_i32 m0, 0x400\n"
    "s_addk_i32 s62, 8192\n"
    "s_waitcnt vmcnt(7)\n"
    "buffer_load_dwordx4 v13, s[44:47], s62 offen lds\n"
    "s_addk_i32 m0, 0x400\n"
    "s_addk_i32 s62, 8192\n"
    "s_waitcnt vmcnt(7)\n"
    "buffer_load_dwordx4 v13, s[44:47], s62 offen lds\n"
    "s_addk_i32 m0, 0x400\n"
    "s_addk_i32 s62, 8192\n"
    "s_waitcnt vmcnt(7)\n"
    "buffer_load_dwordx4 v13, s[44:47], s62 offen lds\n"
    "s_addk_i32 m0, 0x400\n"
    "s_addk_i32 s62, 8192\n"
    "s_add_u32 m0, s63, 32768\n"
    "s_lshl4_add_u32 s62, s26, s54\n"
    "s_waitcnt vmcnt(7)\n"
    "buffer_load_dwordx4 v13, s[44:47], s62 offen lds\n"
    "s_addk_i32 m0, 0x400\n"
    "s_addk_i32 s62, 8192\n"
    "s_waitcnt vmcnt(7)\n"
    "buffer_load_dwordx4 v13, s[44:47], s62 offen lds\n"
    "s_addk_i32 m0, 0x400\n"
    "s_addk_i32 s62, 8192\n"
    "s_waitcnt vmcnt(7)\n"
    "buffer_load_dwordx4 v13, s[44:47], s62 offen lds\n"
    "s_addk_i32 m0, 0x400\n"
    "s_addk_i32 s62, 8192\n"
    "s_waitcnt vmcnt(7)\n"
    "buffer_load_dwordx4 v13, s[44:47], s62 offen lds\n"
    "s_addk_i32 m0, 0x400\n"
    "s_addk_i32 s62, 8192\n"
    "s_add_u32 m0, s63, 49152\n"
    "s_lshl4_add_u32 s62, s27, s54\n"
    "s_waitcnt vmcnt(7)\n"
    "buffer_load_dwordx4 v13, s[44:47], s62 offen lds\n"
    "s_addk_i32 m0, 0x400\n"
    "s_addk_i32 s62, 8192\n"
    "s_waitcnt vmcnt(7)\n"
    "buffer_load_dwordx4 v13, s[44:47], s62 offen lds\n"
    "s_addk_i32 m0, 0x400\n"
    "s_addk_i32 s62, 8192\n"
    "s_waitcnt vmcnt(7)\n"
    "buffer_load_dwordx4 v13, s[44:47], s62 offen lds\n"
    "s_addk_i32 m0, 0x400\n"
    "s_addk_i32 s62, 8192\n"
    "s_waitcnt vmcnt(7)\n"
    "buffer_load_dwordx4 v13, s[44:47], s62 offen lds\n"
    "s_addk_i32 m0, 0x400\n"
    "s_addk_i32 s62, 8192\n"
    "s_add_u32 m0, s63, 65536\n"
    "s_lshl4_add_u32 s62, s52, s54\n"
    "s_waitcnt vmcnt(7)\n"
    "buffer_load_dwordx4 v13, s[44:47], s62 offen lds\n"
    "s_addk_i32 m0, 0x400\n"
    "s_addk_i32 s62, 8192\n"
    "s_waitcnt vmcnt(7)\n"
    "buffer_load_dwordx4 v13, s[44:47], s62 offen lds\n"
    "s_addk_i32 m0, 0x400\n"
    "s_addk_i32 s62, 8192\n"
    "s_waitcnt vmcnt(7)\n"
    "buffer_load_dwordx4 v13, s[44:47], s62 offen lds\n"
    "s_addk_i32 m0, 0x400\n"
    "s_addk_i32 s62, 8192\n"
    "s_waitcnt vmcnt(7)\n"
    "buffer_load_dwordx4 v13, s[44:47], s62 offen lds\n"
    "s_addk_i32 m0, 0x400\n"
    "s_addk_i32 s62, 8192\n"
    "s_add_u32 m0, s63, 81920\n"
    "s_lshl4_add_u32 s62, s53, s54\n"
    "s_waitcnt vmcnt(7)\n"
    "buffer_load_dwordx4 v13, s[44:47], s62 offen lds\n"
    "s_addk_i32 m0, 0x400\n"
    "s_addk_i32 s62, 8192\n"
    "s_waitcnt vmcnt(7)\n"
    "buffer_load_dwordx4 v13, s[44:47], s62 offen lds\n"
    "s_addk_i32 m0, 0x400\n"
    "s_addk_i32 s62, 8192\n"
    "s_waitcnt vmcnt(7)\n"
    "buffer_load_dwordx4 v13, s[44:47], s62 offen lds\n"
    "s_addk_i32 m0, 0x400\n"
    "s_addk_i32 s62, 8192\n"
    "s_waitcnt vmcnt(7)\n"
    "buffer_load_dwordx4 v13, s[44:47], s62 offen lds\n"
    "s_addk_i32 m0, 0x400\n"
    "s_addk_i32 s62, 8192\n"
    "s_add_u32 s60, s60, 330\n"
    ".Lh3_B_sp_delay:\n"
    "s_sleep 2\n"
    "s_memrealtime s[58:59]\n"
    "s_waitcnt lgkmcnt(0)\n"
    "s_sub_u32 s58, s58, s60\n"
    "s_cmp_lt_i32 s58, 0\n"
    "s_cbranch_scc1 .Lh3_B_sp_delay\n"
    "s_setprio 3\n"
    "s_barrier\n"
    "buffer_load_dwordx2 v[28:29], v27, s[28:31], s56 offen sc1\n"
    "s_waitcnt vmcnt(0)\n"
    "s_branch .Lh3_B_join\n"
    ".Lh3_slow_3:\n"
    "s_cmp_eq_u32 s14, 0\n"
    "s_cbranch_scc1 .Lh3_back_4\n"
    "s_sub_u32 s14, s14, 1\n"
    "buffer_load_dwordx2 v[44:45], v121, s[28:31], s62 offen sc1\n"
    "s_waitcnt vmcnt(0)\n"
    "v_xor_b32 v46, s64, v44\n"
    "v_xor_b32 v47, s65, v45\n"
    "v_or_b32 v46, v46, v47\n"
    "v_cmp_ne_u32 vcc, 0, v46\n"
    "s_cbranch_vccnz .Lh3_slow_3\n"
    "s_branch .Lh3_back_4\n"
    ".Lh3_slow_5:\n"
    "s_cmp_eq_u32 s14, 0\n"
    "s_cbranch_scc1 .Lh3_back_6\n"
    "s_sub_u32 s14, s14, 1\n"
    "buffer_load_dwordx4 v[48:51], v6, s[28:31], s63 offen sc1\n"
    "buffer_load_dwordx4 v[52:55], v6, s[28:31], s66 offen sc1\n"
    "s_waitcnt vmcnt(0)\n"
    "v_max3_u32 v46, v48, v49, v50\n"
    "v_max3_u32 v46, v46, v51, v52\n"
    "v_max3_u32 v46, v46, v53, v54\n"
    "v_max_u32 v46, v46, v55\n"
    "v_cmp_eq_u32 vcc, -1, v46\n"
    "s_cbranch_vccnz .Lh3_slow_5\n"
    "s_branch .Lh3_back_6\n"
    ".Lh3_slow_8:\n"
    "s_cmp_eq_u32 s14, 0\n"
    "s_cbranch_scc1 .Lh3_back_9\n"
    "s_sub_u32 s14, s14, 1\n"
    "buffer_load_dwordx4 v[4:7], v3, s[28:31], s58 offen sc1\n"
    "buffer_load_dwordx4 v[8:11], v3, s[28:31], s58 offen offset:256 sc1\n"
    "s_waitcnt vmcnt(0)\n"
    "v_max3_u32 v60, v4, v5, v6\n"
    "v_max3_u32 v60, v60, v7, v8\n"
    "v_max3_u32 v60, v60, v9, v10\n"
    "v_max_u32 v60, v60, v11\n"
    "v_cmp_eq_u32 vcc, -1, v60\n"
    "s_cbranch_vccnz .Lh3_slow_8\n"
    "s_branch .Lh3_back_9\n"
    ".Lh3_slow_10:\n"
    "s_cmp_eq_u32 s14, 0\n"
    "s_cbranch_scc1 .Lh3_back_11\n"
    "s_sub_u32 s14, s14, 1\n"
    "buffer_load_dwordx4 v[16:19], v4, s[28:31], s56 offen sc1\n"
    "buffer_load_dwordx4 v[20:23], v4, s[28:31], s57 offen sc1\n"
    "buffer_load_dword v24, v13, s[28:31], s60 offen sc1\n"
    "buffer_load_dword v26, v13, s[28:31], s60 offen offset:256 sc1\n"
    "s_waitcnt vmcnt(0)\n"
    "v_max3_u32 v12, v16, v17, v18\n"
    "v_max3_u32 v12, v12, v19, v20\n"
    "v_max3_u32 v12, v12, v21, v22\n"
    "v_max3_u32 v12, v12, v23, v24\n"
    "v_max_u32 v12, v12, v26\n"
    "v_cmp_eq_u32 vcc, -1, v12\n"
    "s_cbranch_vccnz .Lh3_slow_10\n"
    "s_branch .Lh3_back_11\n"
    ".Lh3_slow_19:\n"
    "s_cmp_eq_u32 s14, 0\n"
    "s_cbranch_scc1 .Lh3_back_20\n"
    "s_sub_u32 s14, s14, 1\n"
    "buffer_load_dwordx4 v[48:51], v4, s[28:31], s40 offen offset:0 sc1\n"
    "buffer_load_dwordx4 v[52:55], v4, s[28:31], s40 offen offset:512 sc1\n"
    "buffer_load_dwordx4 v[56:59], v4, s[28:31], s40 offen offset:1024 sc1\n"
    "buffer_load_dwordx4 v[60:63], v4, s[28:31], s40 offen offset:1536 sc1\n"
    "buffer_load_dwordx4 v[64:67], v4, s[28:31], s40 offen offset:2048 sc1\n"
    "buffer_load_dwordx4 v[68:71], v4, s[28:31], s40 offen offset:2560 sc1\n"
    "buffer_load_dwordx4 v[72:75], v4, s[28:31], s40 offen offset:3072 sc1\n"
    "buffer_load_dwordx4 v[76:79], v4, s[28:31], s40 offen offset:3584 sc1\n"
    "s_waitcnt vmcnt(0)\n"
    "v_xor_b32 v2, s64, v49\n"
    "v_bitop3_b32 v2, v2, v51, s64 bitop3:0xf6\n"
    "v_bitop3_b32 v2, v2, v53, s64 bitop3:0xf6\n"
    "v_bitop3_b32 v2, v2, v55, s64 bitop3:0xf6\n"
    "v_bitop3_b32 v2, v2, v57, s64 bitop3:0xf6\n"
    "v_bitop3_b32 v2, v2, v59, s64 bitop3:0xf6\n"
    "v_bitop3_b32 v2, v2, v61, s64 bitop3:0xf6\n"
    "v_bitop3_b32 v2, v2, v63, s64 bitop3:0xf6\n"
    "v_bitop3_b32 v2, v2, v65, s64 bitop3:0xf6\n"
    "v_bitop3_b32 v2, v2, v67, s64 bitop3:0xf6\n"
    "v_bitop3_b32 v2, v2, v69, s64 bitop3:0xf6\n"
    "v_bitop3_b32 v2, v2, v71, s64 bitop3:0xf6\n"
    "v_bitop3_b32 v2, v2, v73, s64 bitop3:0xf6\n"
    "v_bitop3_b32 v2, v2, v75, s64 bitop3:0xf6\n"
    "v_bitop3_b32 v2, v2, v77, s64 bitop3:0xf6\n"
    "v_bitop3_b32 v2, v2, v79, s64 bitop3:0xf6\n"
    "v_cmp_ne_u32 vcc, 0, v2\n"
    "s_cbranch_vccnz .Lh3_slow_19\n"
    "s_branch .Lh3_back_20\n"
    ".Lh3_slow_23:\n"
    "s_cmp_eq_u32 s14, 0\n"
    "s_cbranch_scc1 .Lh3_back_24\n"
    "s_sub_u32 s14, s14, 1\n"
    "s_add_u32 m0, s63, 0\n"
    "s_add_u32 s66, s59, 0\n"
    "s_and_b32 s66, s66, 0xffff\n"
    "s_add_u32 s66, s66, s58\n"
    "s_waitcnt vmcnt(7)\n"
    "buffer_load_dwordx4 v5, s[28:31], s66 offen offset:0 sc1 lds\n"
    "s_waitcnt vmcnt(7)\n"
    "buffer_load_dwordx4 v5, s[28:31], s66 offen offset:1024 sc1 lds\n"
    "s_waitcnt vmcnt(7)\n"
    "buffer_load_dwordx4 v5, s[28:31], s66 offen offset:2048 sc1 lds\n"
    "s_waitcnt vmcnt(7)\n"
    "buffer_load_dwordx4 v5, s[28:31], s66 offen offset:3072 sc1 lds\n"
    "s_waitcnt vmcnt(0)\n"
    "ds_read_b128 v[80:83], v4 offset:0\n"
    "ds_read_b128 v[84:87], v4 offset:1024\n"
    "ds_read_b128 v[88:91], v4 offset:2048\n"
    "ds_read_b128 v[92:95], v4 offset:3072\n"
    "s_waitcnt lgkmcnt(0)\n"
    "v_max3_u32 v80, v80, v81, v82\n"
    "v_max3_u32 v83, v83, v84, v85\n"
    "v_max3_u32 v86, v86, v87, v88\n"
    "v_max3_u32 v89, v89, v90, v91\n"
    "v_max3_u32 v92, v92, v93, v94\n"
    "v_max3_u32 v80, v80, v83, v86\n"
    "v_max3_u32 v89, v89, v92, v95\n"
    "v_max_u32 v80, v80, v89\n"
    "v_cmp_eq_u32 vcc, -1, v80\n"
    "s_cbranch_vccnz .Lh3_slow_23\n"
    "s_branch .Lh3_back_24\n"
    ".Lh3_slow_25:\n"
    "s_cmp_eq_u32 s14, 0\n"
    "s_cbranch_scc1 .Lh3_back_26\n"
    "s_sub_u32 s14, s14, 1\n"
    "s_add_u32 m0, s63, 16384\n"
    "s_add_u32 s66, s59, 16384\n"
    "s_and_b32 s66, s66, 0xffff\n"
    "s_add_u32 s66, s66, s58\n"
    "s_waitcnt vmcnt(7)\n"
    "buffer_load_dwordx4 v5, s[28:31], s66 offen offset:0 sc1 lds\n"
    "s_waitcnt vmcnt(7)\n"
    "buffer_load_dwordx4 v5, s[28:31], s66 offen offset:1024 sc1 lds\n"
    "s_waitcnt vmcnt(7)\n"
    "buffer_load_dwordx4 v5, s[28:31], s66 offen offset:2048 sc1 lds\n"
    "s_waitcnt vmcnt(7)\n"
    "buffer_load_dwordx4 v5, s[28:31], s66 offen offset:3072 sc1 lds\n"
    "s_waitcnt vmcnt(0)\n"
    "ds_read_b128 v[96:99], v4 offset:16384\n"
    "ds_read_b128 v[100:103], v4 offset:17408\n"
    "ds_read_b128 v[104:107], v4 offset:18432\n"
    "ds_read_b128 v[108:111], v4 offset:19456\n"
    "s_waitcnt lgkmcnt(0)\n"
    "v_max3_u32 v96, v96, v97, v98\n"
    "v_max3_u32 v99, v99, v100, v101\n"
    "v_max3_u32 v102, v102, v103, v104\n"
    "v_max3_u32 v105, v105, v106, v107\n"
    "v_max3_u32 v108, v108, v109, v110\n"
    "v_max3_u32 v96, v96, v99, v102\n"
    "v_max3_u32 v105, v105, v108, v111\n"
    "v_max_u32 v96, v96, v105\n"
    "v_cmp_eq_u32 vcc, -1, v96\n"
    "s_cbranch_vccnz .Lh3_slow_25\n"
    "s_branch .Lh3_back_26\n"
    ".Lh3_slow_27:\n"
    "s_cmp_eq_u32 s14, 0\n"
    "s_cbranch_scc1 .Lh3_back_28\n"
    "s_sub_u32 s14, s14, 1\n"
    "s_add_u32 m0, s63, 32768\n"
    "s_add_u32 s66, s59, 32768\n"
    "s_and_b32 s66, s66, 0xffff\n"
    "s_add_u32 s66, s66, s58\n"
    "s_waitcnt vmcnt(7)\n"
    "buffer_load_dwordx4 v5, s[28:31], s66 offen offset:0 sc1 lds\n"
    "s_waitcnt vmcnt(7)\n"
    "buffer_load_dwordx4 v5, s[28:31], s66 offen offset:1024 sc1 lds\n"
    "s_waitcnt vmcnt(7)\n"
    "buffer_load_dwordx4 v5, s[28:31], s66 offen offset:2048 sc1 lds\n"
    "s_waitcnt vmcnt(7)\n"
    "buffer_load_dwordx4 v5, s[28:31], s66 offen offset:3072 sc1 lds\n"
    "s_waitcnt vmcnt(0)\n"
    "ds_read_b128 v[112:115], v4 offset:32768\n"
    "ds_read_b128 v[116:119], v4 offset:33792\n"
    "ds_read_b128 v[120:123], v4 offset:34816\n"
    "ds_read_b128 v[124:127], v4 offset:35840\n"
    "s_waitcnt lgkmcnt(0)\n"
    "v_max3_u32 v112, v112, v113, v114\n"
    "v_max3_u32 v115, v115, v116, v117\n"
    "v_max3_u32 v118, v118, v119, v120\n"
    "v_max3_u32 v121, v121, v122, v123\n"
    "v_max3_u32 v124, v124, v125, v126\n"
    "v_max3_u32 v112, v112, v115, v118\n"
    "v_max3_u32 v121, v121, v124, v127\n"
    "v_max_u32 v112, v112, v121\n"
    "v_cmp_eq_u32 vcc, -1, v112\n"
    "s_cbranch_vccnz .Lh3_slow_27\n"
    "s_branch .Lh3_back_28\n"
    ".Lh3_slow_29:\n"
    "s_cmp_eq_u32 s14, 0\n"
    "s_cbranch_scc1 .Lh3_back_30\n"
    "s_sub_u32 s14, s14, 1\n"
    "s_add_u32 m0, s63, 49152\n"
    "s_add_u32 s66, s59, 49152\n"
    "s_and_b32 s66, s66, 0xffff\n"
    "s_add_u32 s66, s66, s58\n"
    "s_waitcnt vmcnt(7)\n"
    "buffer_load_dwordx4 v5, s[28:31], s66 offen offset:0 sc1 lds\n"
    "s_waitcnt vmcnt(7)\n"
    "buffer_load_dwordx4 v5, s[28:31], s66 offen offset:1024 sc1 lds\n"
    "s_waitcnt vmcnt(7)\n"
    "buffer_load_dwordx4 v5, s[28:31], s66 offen offset:2048 sc1 lds\n"
    "s_waitcnt vmcnt(7)\n"
    "buffer_load_dwordx4 v5, s[28:31], s66 offen offset:3072 sc1 lds\n"
    "s_waitcnt vmcnt(0)\n"
    "ds_read_b128 v[80:83], v4 offset:49152\n"
    "ds_read_b128 v[84:87], v4 offset:50176\n"
    "ds_read_b128 v[88:91], v4 offset:51200\n"
    "ds_read_b128 v[92:95], v4 offset:52224\n"
    "s_waitcnt lgkmcnt(0)\n"
    "v_max3_u32 v80, v80, v81, v82\n"
    "v_max3_u32 v83, v83, v84, v85\n"
    "v_max3_u32 v86, v86, v87, v88\n"
    "v_max3_u32 v89, v89, v90, v91\n"
    "v_max3_u32 v92, v92, v93, v94\n"
    "v_max3_u32 v80, v80, v83, v86\n"
    "v_max3_u32 v89, v89, v92, v95\n"
    "v_max_u32 v80, v80, v89\n"
    "v_cmp_eq_u32 vcc, -1, v80\n"
    "s_cbranch_vccnz .Lh3_slow_29\n"
    "s_branch .Lh3_back_30\n"
    ".Lh3_slow_31:\n"
    "s_cmp_eq_u32 s14, 0\n"
    "s_cbranch_scc1 .Lh3_back_32\n"
    "s_sub_u32 s14, s14, 1\n"
    "s_add_u32 m0, s63, 65536\n"
    "s_add_u32 s66, s59, 0\n"
    "s_and_b32 s66, s66, 0xffff\n"
    "s_add_u32 s66, s66, s58\n"
    "s_add_u32 s66, s66, 65536\n"
    "s_waitcnt vmcnt(7)\n"
    "buffer_load_dwordx4 v5, s[28:31], s66 offen offset:0 sc1 lds\n"
    "s_waitcnt vmcnt(7)\n"
    "buffer_load_dwordx4 v5, s[28:31], s66 offen offset:1024 sc1 lds\n"
    "s_waitcnt vmcnt(7)\n"
    "buffer_load_dwordx4 v5, s[28:31], s66 offen offset:2048 sc1 lds\n"
    "s_waitcnt vmcnt(7)\n"
    "buffer_load_dwordx4 v5, s[28:31], s66 offen offset:3072 sc1 lds\n"
    "s_waitcnt vmcnt(0)\n"
    "ds_read_b128 v[96:99], v18 offset:0\n"
    "ds_read_b128 v[100:103], v18 offset:1024\n"
    "ds_read_b128 v[104:107], v18 offset:2048\n"
    "ds_read_b128 v[108:111], v18 offset:3072\n"
    "s_waitcnt lgkmcnt(0)\n"
    "v_max3_u32 v96, v96, v97, v98\n"
    "v_max3_u32 v99, v99, v100, v101\n"
    "v_max3_u32 v102, v102, v103, v104\n"
    "v_max3_u32 v105, v105, v106, v107\n"
    "v_max3_u32 v108, v108, v109, v110\n"
    "v_max3_u32 v96, v96, v99, v102\n"
    "v_max3_u32 v105, v105, v108, v111\n"
    "v_max_u32 v96, v96, v105\n"
    "v_cmp_eq_u32 vcc, -1, v96\n"
    "s_cbranch_vccnz .Lh3_slow_31\n"
    "s_branch .Lh3_back_32\n"
    ".Lh3_slow_33:\n"
    "s_cmp_eq_u32 s14, 0\n"
    "s_cbranch_scc1 .Lh3_back_34\n"
    "s_sub_u32 s14, s14, 1\n"
    "s_add_u32 m0, s63, 81920\n"
    "s_add_u32 s66, s59, 16384\n"
    "s_and_b32 s66, s66, 0xffff\n"
    "s_add_u32 s66, s66, s58\n"
    "s_add_u32 s66, s66, 65536\n"
    "s_waitcnt vmcnt(7)\n"
    "buffer_load_dwordx4 v5, s[28:31], s66 offen offset:0 sc1 lds\n"
    "s_waitcnt vmcnt(7)\n"
    "buffer_load_dwordx4 v5, s[28:31], s66 offen offset:1024 sc1 lds\n"
    "s_waitcnt vmcnt(7)\n"
    "buffer_load_dwordx4 v5, s[28:31], s66 offen offset:2048 sc1 lds\n"
    "s_waitcnt vmcnt(7)\n"
    "buffer_load_dwordx4 v5, s[28:31], s66 offen offset:3072 sc1 lds\n"
    "s_waitcnt vmcnt(0)\n"
    "ds_read_b128 v[112:115], v18 offset:16384\n"
    "ds_read_b128 v[116:119], v18 offset:17408\n"
    "ds_read_b128 v[120:123], v18 offset:18432\n"
    "ds_read_b128 v[124:127], v18 offset:19456\n"
    "s_waitcnt lgkmcnt(0)\n"
    "v_max3_u32 v112, v112, v113, v114\n"
    "v_max3_u32 v115, v115, v116, v117\n"
    "v_max3_u32 v118, v118, v119, v120\n"
    "v_max3_u32 v121, v121, v122, v123\n"
    "v_max3_u32 v124, v124, v125, v126\n"
    "v_max3_u32 v112, v112, v115, v118\n"
    "v_max3_u32 v121, v121, v124, v127\n"
    "v_max_u32 v112, v112, v121\n"
    "v_cmp_eq_u32 vcc, -1, v112\n"
    "s_cbranch_vccnz .Lh3_slow_33\n"
    "s_branch .Lh3_back_34\n"
    ".Lh3_slow_35:\n"
    "s_cmp_eq_u32 s14, 0\n"
    "s_cbranch_scc1 .Lh3_back_36\n"
    "s_sub_u32 s14, s14, 1\n"
    "s_add_u32 m0, s63, 98304\n"
    "s_add_u32 s66, s59, 32768\n"
    "s_and_b32 s66, s66, 0xffff\n"
    "s_add_u32 s66, s66, s58\n"
    "s_add_u32 s66, s66, 65536\n"
    "s_waitcnt vmcnt(7)\n"
    "buffer_load_dwordx4 v5, s[28:31], s66 offen offset:0 sc1 lds\n"
    "s_waitcnt vmcnt(7)\n"
    "buffer_load_dwordx4 v5, s[28:31], s66 offen offset:1024 sc1 lds\n"
    "s_waitcnt vmcnt(7)\n"
    "buffer_load_dwordx4 v5, s[28:31], s66 offen offset:2048 sc1 lds\n"
    "s_waitcnt vmcnt(7)\n"
    "buffer_load_dwordx4 v5, s[28:31], s66 offen offset:3072 sc1 lds\n"
    "s_waitcnt vmcnt(0)\n"
    "ds_read_b128 v[80:83], v18 offset:32768\n"
    "ds_read_b128 v[84:87], v18 offset:33792\n"
    "ds_read_b128 v[88:91], v18 offset:34816\n"
    "ds_read_b128 v[92:95], v18 offset:35840\n"
    "s_waitcnt lgkmcnt(0)\n"
    "v_max3_u32 v80, v80, v81, v82\n"
    "v_max3_u32 v83, v83, v84, v85\n"
    "v_max3_u32 v86, v86, v87, v88\n"
    "v_max3_u32 v89, v89, v90, v91\n"
    "v_max3_u32 v92, v92, v93, v94\n"
    "v_max3_u32 v80, v80, v83, v86\n"
    "v_max3_u32 v89, v89, v92, v95\n"
    "v_max_u32 v80, v80, v89\n"
    "v_cmp_eq_u32 vcc, -1, v80\n"
    "s_cbranch_vccnz .Lh3_slow_35\n"
    "s_branch .Lh3_back_36\n"
    ".Lh3_slow_37:\n"
    "s_cmp_eq_u32 s14, 0\n"
    "s_cbranch_scc1 .Lh3_back_38\n"
    "s_sub_u32 s14, s14, 1\n"
    "s_add_u32 m0, s63, 114688\n"
    "s_add_u32 s66, s59, 49152\n"
    "s_and_b32 s66, s66, 0xffff\n"
    "s_add_u32 s66, s66, s58\n"
    "s_add_u32 s66, s66, 65536\n"
    "s_waitcnt vmcnt(7)\n"
    "buffer_load_dwordx4 v5, s[28:31], s66 offen offset:0 sc1 lds\n"
    "s_waitcnt vmcnt(7)\n"
    "buffer_load_dwordx4 v5, s[28:31], s66 offen offset:1024 sc1 lds\n"
    "s_waitcnt vmcnt(7)\n"
    "buffer_load_dwordx4 v5, s[28:31], s66 offen offset:2048 sc1 lds\n"
    "s_waitcnt vmcnt(7)\n"
    "buffer_load_dwordx4 v5, s[28:31], s66 offen offset:3072 sc1 lds\n"
    "s_waitcnt vmcnt(0)\n"
    "ds_read_b128 v[96:99], v18 offset:49152\n"
    "ds_read_b128 v[100:103], v18 offset:50176\n"
    "ds_read_b128 v[104:107], v18 offset:51200\n"
    "ds_read_b128 v[108:111], v18 offset:52224\n"
    "s_waitcnt lgkmcnt(0)\n"
    "v_max3_u32 v96, v96, v97, v98\n"
    "v_max3_u32 v99, v99, v100, v101\n"
    "v_max3_u32 v102, v102, v103, v104\n"
    "v_max3_u32 v105, v105, v106, v107\n"
    "v_max3_u32 v108, v108, v109, v110\n"
    "v_max3_u32 v96, v96, v99, v102\n"
    "v_max3_u32 v105, v105, v108, v111\n"
    "v_max_u32 v96, v96, v105\n"
    "v_cmp_eq_u32 vcc, -1, v96\n"
    "s_cbranch_vccnz .Lh3_slow_37\n"
    "s_branch .Lh3_back_38\n"
    ".Lh3_slow_39:\n"
    "s_cmp_eq_u32 s14, 0\n"
    "s_cbranch_scc1 .Lh3_back_40\n"
    "s_sub_u32 s14, s14, 1\n"
    "s_sleep 1\n"
    "buffer_load_dwordx2 v[28:29], v27, s[28:31], s56 offen sc1\n"
    "s_waitcnt vmcnt(0)\n"
    "v_xor_b32 v28, s64, v28\n"
    "v_xor_b32 v29, s65, v29\n"
    "v_or_b32 v28, v28, v29\n"
    "v_cmp_ne_u32 vcc, 0, v28\n"
    "s_cbranch_vccnz .Lh3_slow_39\n"
    "s_branch .Lh3_back_40\n"
    ".Lh3_slow_41:\n"
    "s_cmp_eq_u32 s14, 0\n"
    "s_cbranch_scc1 .Lh3_back_42\n"
    "s_sub_u32 s14, s14, 1\n"
    "s_sleep 1\n"
    "buffer_load_dwordx2 v[28:29], v27, s[28:31], s57 offen sc1\n"
    "s_waitcnt vmcnt(0)\n"
    "v_xor_b32 v28, s64, v28\n"
    "v_xor_b32 v29, s65, v29\n"
    "v_or_b32 v28, v28, v29\n"
    "v_cmp_ne_u32 vcc, 0, v28\n"
    "s_cbranch_vccnz .Lh3_slow_41\n"
    "s_branch .Lh3_back_42\n"
    ".Lh3_slow_43:\n"
    "s_cmp_eq_u32 s14, 0\n"
    "s_cbranch_scc1 .Lh3_back_44\n"
    "s_sub_u32 s14, s14, 1\n"
    "s_add_u32 m0, s63, 0\n"
    "s_add_u32 s66, s59, 0\n"
    "s_and_b32 s66, s66, 0xffff\n"
    "s_add_u32 s66, s66, s58\n"
    "s_waitcnt vmcnt(7)\n"
    "buffer_load_dwordx4 v5, s[28:31], s66 offen offset:0 sc1 lds\n"
    "s_waitcnt vmcnt(7)\n"
    "buffer_load_dwordx4 v5, s[28:31], s66 offen offset:1024 sc1 lds\n"
    "s_waitcnt vmcnt(7)\n"
    "buffer_load_dwordx4 v5, s[28:31], s66 offen offset:2048 sc1 lds\n"
    "s_waitcnt vmcnt(7)\n"
    "buffer_load_dwordx4 v5, s[28:31], s66 offen offset:3072 sc1 lds\n"
    "s_waitcnt vmcnt(0)\n"
    "ds_read_b64_tr_b16 v[80:81], v8 offset:0\n"
    "ds_read_b64_tr_b16 v[82:83], v8 offset:2048\n"
    "ds_read_b64_tr_b16 v[84:85], v9 offset:0\n"
    "ds_read_b64_tr_b16 v[86:87], v9 offset:2048\n"
    "ds_read_b64_tr_b16 v[88:89], v10 offset:0\n"
    "ds_read_b64_tr_b16 v[90:91], v10 offset:2048\n"
    "ds_read_b64_tr_b16 v[92:93], v11 offset:0\n"
    "ds_read_b64_tr_b16 v[94:95], v11 offset:2048\n"
    "s_waitcnt lgkmcnt(0)\n"
    "v_dot2_f32_bf16 v104, v80, v19, 0\n"
    "v_dot2_f32_bf16 v106, v84, v19, 0\n"
    "v_dot2_f32_bf16 v108, v88, v19, 0\n"
    "v_dot2_f32_bf16 v110, v92, v19, 0\n"
    "v_dot2_f32_bf16 v104, v81, v19, v104\n"
    "v_dot2_f32_bf16 v106, v85, v19, v106\n"
    "v_dot2_f32_bf16 v108, v89, v19, v108\n"
    "v_dot2_f32_bf16 v110, v93, v19, v110\n"
    "v_dot2_f32_bf16 v104, v82, v19, v104\n"
    "v_dot2_f32_bf16 v106, v86, v19, v106\n"
    "v_dot2_f32_bf16 v108, v90, v19, v108\n"
    "v_dot2_f32_bf16 v110, v94, v19, v110\n"
    "v_dot2_f32_bf16 v104, v83, v19, v104\n"
    "v_dot2_f32_bf16 v106, v87, v19, v106\n"
    "v_dot2_f32_bf16 v108, v91, v19, v108\n"
    "v_dot2_f32_bf16 v110, v95, v19, v110\n"
    "s_nop 1\n"
    "v_cmp_u_f32 vcc, v104, v106\n"
    "v_cmp_u_f32 s[66:67], v108, v110\n"
    "s_or_b64 vcc, vcc, s[66:67]\n"
    "s_cbranch_vccnz .Lh3_slow_43\n"
    "s_branch .Lh3_back_44\n"
    ".Lh3_slow_45:\n"
    "s_cmp_eq_u32 s14, 0\n"
    "s_cbranch_scc1 .Lh3_back_46\n"
    "s_sub_u32 s14, s14, 1\n"
    "s_add_u32 m0, s63, 16384\n"
    "s_add_u32 s66, s59, 16384\n"
    "s_and_b32 s66, s66, 0xffff\n"
    "s_add_u32 s66, s66, s58\n"
    "s_waitcnt vmcnt(7)\n"
    "buffer_load_dwordx4 v5, s[28:31], s66 offen offset:0 sc1 lds\n"
    "s_waitcnt vmcnt(7)\n"
    "buffer_load_dwordx4 v5, s[28:31], s66 offen offset:1024 sc1 lds\n"
    "s_waitcnt vmcnt(7)\n"
    "buffer_load_dwordx4 v5, s[28:31], s66 offen offset:2048 sc1 lds\n"
    "s_waitcnt vmcnt(7)\n"
    "buffer_load_dwordx4 v5, s[28:31], s66 offen offset:3072 sc1 lds\n"
    "s_waitcnt vmcnt(0)\n"
    "ds_read_b64_tr_b16 v[80:81], v8 offset:16384\n"
    "ds_read_b64_tr_b16 v[82:83], v8 offset:18432\n"
    "ds_read_b64_tr_b16 v[84:85], v9 offset:16384\n"
    "ds_read_b64_tr_b16 v[86:87], v9 offset:18432\n"
    "ds_read_b64_tr_b16 v[88:89], v10 offset:16384\n"
    "ds_read_b64_tr_b16 v[90:91], v10 offset:18432\n"
    "ds_read_b64_tr_b16 v[92:93], v11 offset:16384\n"
    "ds_read_b64_tr_b16 v[94:95], v11 offset:18432\n"
    "s_waitcnt lgkmcnt(0)\n"
    "v_dot2_f32_bf16 v96, v80, v19, v104\n"
    "v_dot2_f32_bf16 v98, v84, v19, v106\n"
    "v_dot2_f32_bf16 v100, v88, v19, v108\n"
    "v_dot2_f32_bf16 v102, v92, v19, v110\n"
    "v_dot2_f32_bf16 v96, v81, v19, v96\n"
    "v_dot2_f32_bf16 v98, v85, v19, v98\n"
    "v_dot2_f32_bf16 v100, v89, v19, v100\n"
    "v_dot2_f32_bf16 v102, v93, v19, v102\n"
    "v_dot2_f32_bf16 v96, v82, v19, v96\n"
    "v_dot2_f32_bf16 v98, v86, v19, v98\n"
    "v_dot2_f32_bf16 v100, v90, v19, v100\n"
    "v_dot2_f32_bf16 v102, v94, v19, v102\n"
    "v_dot2_f32_bf16 v96, v83, v19, v96\n"
    "v_dot2_f32_bf16 v98, v87, v19, v98\n"
    "v_dot2_f32_bf16 v100, v91, v19, v100\n"
    "v_dot2_f32_bf16 v102, v95, v19, v102\n"
    "s_nop 1\n"
    "v_cmp_u_f32 vcc, v96, v98\n"
    "v_cmp_u_f32 s[66:67], v100, v102\n"
    "s_or_b64 vcc, vcc, s[66:67]\n"
    "s_cbranch_vccnz .Lh3_slow_45\n"
    "s_branch .Lh3_back_46\n"
    ".Lh3_KEND:\n"
    :
    : "{s[0:1]}"(__builtin_amdgcn_kernarg_segment_ptr()), "{s2}"(blockIdx.x), "{v0}"(threadIdx.x)
    : "memory", "vcc", "scc", "v1", "v2", "v3", "v4", "v5", "v6", "v7", "v8", "v9", "v10", "v11", "v12", "v13", "v14", "v15", "v16", "v17", "v18", "v19", "v20", "v21", "v22", "v23", "v24", "v25", "v26", "v27", "v28", "v29", "v30", "v31", "v32", "v33", "v34", "v35", "v36", "v37", "v38", "v39", "v40", "v41", "v42", "v43", "v44", "v45", "v46", "v47", "v48", "v49", "v50", "v51", "v52", "v53", "v54", "v55", "v56", "v57", "v58", "v59", "v60", "v61", "v62", "v63", "v64", "v65", "v66", "v67", "v68", "v69", "v70", "v71", "v72", "v73", "v74", "v75", "v76", "v77", "v78", "v79", "v80", "v81", "v82", "v83", "v84", "v85", "v86", "v87", "v88", "v89", "v90", "v91", "v92", "v93", "v94", "v95", "v96", "v97", "v98", "v99", "v100", "v101", "v102", "v103", "v104", "v105", "v106", "v107", "v108", "v109", "v110", "v111", "v112", "v113", "v114", "v115", "v116", "v117", "v118", "v119", "v120", "v121", "v122", "v123", "v124", "v125", "v126", "v127", "v128", "v129", "v130", "v131", "v132", "v133", "v134", "v135", "v136", "v137", "v138", "v139", "v140", "v141", "v142", "v143", "v144", "v145", "v146", "v147", "v148", "v149", "v150", "v151", "v152", "v153", "v154", "v155", "v156", "v157", "v158", "v159", "v160", "v161", "v162", "v163", "v164", "v165", "v166", "v167", "v168", "v169", "v170", "v171", "v172", "v173", "v174", "v175", "v176", "v177", "v178", "v179", "v180", "v181", "v182", "v183", "v184", "v185", "v186", "v187", "v188", "v189", "v190", "v191", "v192", "v193", "v194", "v195", "v196", "v197", "v198", "v199", "v200", "v201", "v202", "v203", "v204", "v205", "v206", "v207", "v208", "v209", "v210", "v211", "v212", "v213", "v214", "v215", "v216", "v217", "v218", "v219", "v220", "v221", "v222", "v223", "v224", "v225", "v226", "v227", "v228", "v229", "v230", "v231", "v232", "v233", "v234", "v235", "v236", "v237", "v238", "v239", "v240", "v241", "v242", "v243", "v244", "v245", "v246", "v247", "v248", "v249", "v250", "v251", "v252", "v253", "v254", "v255", "s3", "s4", "s5", "s6", "s7", "s8", "s9", "s10", "s11", "s12", "s13", "s14", "s15", "s16", "s17", "s18", "s19", "s20", "s21", "s22", "s23", "s24", "s25", "s26", "s27", "s28", "s29", "s30", "s31", "s33", "s34", "s35", "s36", "s37", "s38", "s39", "s40", "s41", "s42", "s43", "s44", "s45", "s46", "s47", "s48", "s49", "s50", "s51", "s52", "s53", "s54", "s55", "s56", "s57", "s58", "s59", "s60", "s61", "s62", "s63", "s64", "s65", "s66", "s67", "s68", "s69", "s70", "s71", "s72", "s73", "s74", "s75", "s76", "s77", "s78", "s79", "s80", "s81", "s82", "s83", "s84", "s85", "s86", "s87", "s88", "s89", "s90", "s91", "s92", "s93", "s94", "s95", "s96", "s97", "s98", "s99");
}

extern "C" void kernel_launch(void* const* d_in, const int* in_sizes, int n_in, void* d_out, int out_size, void* d_ws, size_t ws_size,
                              hipStream_t stream) {
  constexpr unsigned FMT = 0x00020000u, MB2 = 2u << 20, WB = 512u * 1792u * 2u;
  (void)hipFuncSetAttribute((const void*)h3_ffn_mega, hipFuncAttributeMaxDynamicSharedMemorySize, 163840);
  h3_ffn_mega<<<256, 512, 163840, stream>>>(d_in[1], MB2, FMT, d_in[2], MB2, FMT, d_in[3], 4096u, FMT, d_ws, 14090240u, FMT,
                                            d_in[0], MB2, FMT, d_in[4], MB2, FMT, d_in[5], 1024u, FMT, d_in[8], WB, FMT,
                                            d_in[7], WB, FMT, d_out, MB2, FMT);
}
```
